```python
import torch

def _softmax(x, axis=-1):
    return torch.softmax(x, dim=axis)
def _gelu(x):
    return torch.nn.functional.gelu(x, approximate="tanh")
def _silu(x):
    return torch.nn.functional.silu(x)
def _sigmoid(x):
    return torch.sigmoid(x)
def _layernorm(x, axis=-1, eps=1e-5):
    m = x.mean(dim=axis, keepdim=True)
    v = x.var(dim=axis, keepdim=True, unbiased=False)
    return (x - m) / torch.sqrt(v + eps)
def _scatter_add(tgt, idx, src, axis=0):
    return tgt.clone().index_add_(axis, idx, src)
def _segment_sum(src, seg, n):
    out = torch.zeros((n,) + src.shape[1:], dtype=src.dtype)
    return out.index_add_(0, seg, src)
def _pad(x, pw):
    flat = tuple(v for pair in reversed(pw) for v in pair)
    return torch.nn.functional.pad(x, flat)

kernel_name = 'sequential_preLN__softmax_full_mha__glu_gated_swiglu__prompt'

def setup_inputs():

    _w = (lambda a: (lambda t: t.to(torch.bfloat16) if t.is_floating_point() else t)(torch.as_tensor(a)))
    def _A_setup():
        B, S, D = 1, 2048, 512
        return {
            "x": _w((lambda *s: torch.randn(*s, dtype=torch.bfloat16) * 0.05)(B, S, D)),
            "wqkv": _w((lambda *s: torch.randn(*s, dtype=torch.bfloat16) * 0.05)(D, 3 * D)),
            "wo": _w((lambda *s: torch.randn(*s, dtype=torch.bfloat16) * 0.05)(D, D)),
        }
    _A_in = _A_setup()

    def _B_setup():
        B, S, D, F = 1, 2048, 512, 1376
        return {
            "x": _w((lambda *s: torch.randn(*s, dtype=torch.bfloat16) * 0.05)(B, S, D)),
            "w1": _w((lambda *s: torch.randn(*s, dtype=torch.bfloat16) * 0.4)(D, F) * (2.0 / D) ** 0.5),
            "w3": _w((lambda *s: torch.randn(*s, dtype=torch.bfloat16) * 0.4)(D, F) * (2.0 / D) ** 0.5),
            "w2": _w((lambda *s: torch.randn(*s, dtype=torch.bfloat16) * 0.4)(F, D) * (2.0 / F) ** 0.5),
        }
    _B_in = _B_setup()

    return {
        "A_x": _A_in["x"],
        "A_wqkv": _A_in["wqkv"],
        "A_wo": _A_in["wo"],
        "B_x": _B_in["x"],
        "B_w1": _B_in["w1"],
        "B_w3": _B_in["w3"],
        "B_w2": _B_in["w2"],
    }

def reference(A_x, A_wqkv, A_wo, B_x, B_w1, B_w3, B_w2):
    def _A_ref(x, wqkv, wo):
        B, S, D = x.shape
        H = 32; Dh = D // H
        qkv = (lambda x, *d: x.permute(*d))((x @ wqkv).reshape(B, S, 3, H, Dh), 2, 0, 3, 1, 4)
        q, k, v = qkv[0], qkv[1], qkv[2]
        scale = Dh ** -0.5
        scores = torch.einsum("bhqd,bhkd->bhqk", q, k) * scale
        p = _softmax(scores, axis=-1)
        o = torch.einsum("bhqk,bhkd->bhqd", p, v)
        return (lambda x, *d: x.permute(*d))(o, 0, 2, 1, 3).reshape(B, S, D) @ wo

    def _B_ref(x, w1, w3, w2):
        gate = torch.einsum("bsd,df->bsf", x, w1)
        up = torch.einsum("bsd,df->bsf", x, w3)
        h = _silu(gate) * up
        out = torch.einsum("bsf,fd->bsd", h, w2)
        return out

    x = A_x
    h = x + _A_ref(_layernorm(x, axis=-1), A_wqkv, A_wo)
    h2 = h + _B_ref(_layernorm(h, axis=-1), B_w1, B_w3, B_w2)
    return h2
```

```cpp
#include <hip/hip_runtime.h>
#include <hip/hip_bf16.h>
#include <stdint.h>
typedef unsigned short u16;
__device__ __forceinline__ float bf2f(u16 b){ return __uint_as_float(((unsigned)b)<<16); }
__device__ __forceinline__ u16 f2bf(float f){ unsigned u=__float_as_uint(f); u += 0x7fffu + ((u>>16)&1u); return (u16)(u>>16); }
constexpr int S=2048, D=512, H=32, DH=16, F=1376;
template<bool INF32> __global__ void ln_k(const void* in, float* out){
  int row=blockIdx.x; int t=threadIdx.x;
  float v[8]; float s=0;
  for(int j=0;j<8;j++){ int c=t*8+j; v[j]= INF32? ((const float*)in)[row*D+c] : bf2f(((const u16*)in)[row*D+c]); s+=v[j]; }
  for(int o=32;o;o>>=1) s+=__shfl_xor(s,o);
  float m=s/D; float q=0; for(int j=0;j<8;j++){ float d=v[j]-m; q+=d*d; }
  for(int o=32;o;o>>=1) q+=__shfl_xor(q,o);
  float r=1.0f/sqrtf(q/D+1e-5f);
  for(int j=0;j<8;j++) out[row*D+t*8+j]=(v[j]-m)*r;
}
__global__ void gemm_k(const float* A,const u16* W,float* C,int M,int N,int K){
  __shared__ float As[16][17]; __shared__ float Ws[16][17];
  int tx=threadIdx.x, ty=threadIdx.y; int r=blockIdx.y*16+ty, c=blockIdx.x*16+tx; float acc=0;
  for(int k0=0;k0<K;k0+=16){ As[ty][tx]=A[r*K+k0+tx]; Ws[ty][tx]=bf2f(W[(k0+ty)*N+c]); __syncthreads();
    for(int k=0;k<16;k++) acc+=As[ty][k]*Ws[k][tx]; __syncthreads(); }
  C[r*N+c]=acc;
}
__global__ void attn_k(const float* qkv,float* o){
  int h=blockIdx.y; int q=blockIdx.x*64+threadIdx.x; float qr[16]; for(int d=0;d<16;d++) qr[d]=qkv[q*1536+h*16+d]*0.25f;
  float m=-1e30f,l=0,acc[16]; for(int d=0;d<16;d++) acc[d]=0;
  for(int k=0;k<S;k++){ float s=0; for(int d=0;d<16;d++) s+=qr[d]*qkv[k*1536+512+h*16+d];
    float mn=fmaxf(m,s); float a=__expf(m-mn), p=__expf(s-mn); l=l*a+p; for(int d=0;d<16;d++) acc[d]=acc[d]*a+p*qkv[k*1536+1024+h*16+d]; m=mn; }
  for(int d=0;d<16;d++) o[q*D+h*16+d]=acc[d]/l;
}
__global__ void addx_k(const u16* x,float* h){ int i=blockIdx.x*256+threadIdx.x; h[i]+=bf2f(x[i]); }
__global__ void swi_k(float* g,const float* u){ int i=blockIdx.x*256+threadIdx.x; float a=g[i]; g[i]=a/(1.f+__expf(-a))*u[i]; }
__global__ void fin_k(const float* h,const float* y,u16* out){ int i=blockIdx.x*256+threadIdx.x; out[i]=f2bf(h[i]+y[i]); }
extern "C" void kernel_launch(void* const* d_in, const int* in_sizes, int n_in, void* d_out, int out_size, void* d_ws, size_t ws_size, hipStream_t stream){
  const u16* x=(const u16*)d_in[0]; const u16* wqkv=(const u16*)d_in[1]; const u16* wo=(const u16*)d_in[2];
  const u16* w1=(const u16*)d_in[4]; const u16* w3=(const u16*)d_in[5]; const u16* w2=(const u16*)d_in[6];
  float* ws=(float*)d_ws; float* xn=ws; float* qkv=xn+S*D; float* o=qkv+S*1536; float* h=o+S*D; float* g=h+S*D; float* u=g+S*F; float* y=u+S*F;
  ln_k<false><<<S,64,0,stream>>>(x,xn);
  gemm_k<<<dim3(1536/16,S/16),dim3(16,16),0,stream>>>(xn,wqkv,qkv,S,1536,D);
  attn_k<<<dim3(S/64,H),64,0,stream>>>(qkv,o);
  gemm_k<<<dim3(D/16,S/16),dim3(16,16),0,stream>>>(o,wo,h,S,D,D);
  addx_k<<<S*D/256,256,0,stream>>>(x,h);
  ln_k<true><<<S,64,0,stream>>>(h,xn);
  gemm_k<<<dim3(F/16,S/16),dim3(16,16),0,stream>>>(xn,w1,g,S,F,D);
  gemm_k<<<dim3(F/16,S/16),dim3(16,16),0,stream>>>(xn,w3,u,S,F,D);
  swi_k<<<S*F/256,256,0,stream>>>(g,u);
  gemm_k<<<dim3(D/16,S/16),dim3(16,16),0,stream>>>(g,w2,y,S,D,F);
  fin_k<<<S*D/256,256,0,stream>>>(h,y,(u16*)d_out);
}
```

```cpp
#include <hip/hip_runtime.h>
#pragma clang diagnostic ignored "-Winline-asm"

#define FBK_ASM \
    \
  "s_load_dwordx2 s[4:5], s[0:1], 0x0\n" \
  "s_load_dwordx2 s[8:9], s[0:1], 0x8\n" \
  "v_readfirstlane_b32 s36, v0\n" \
  "v_and_b32 v250, 63, v0\n" \
  "s_load_dwordx2 s[12:13], s[0:1], 0x38\n" \
  "s_load_dwordx2 s[20:21], s[0:1], 0x10\n" \
  "s_lshr_b32 s36, s36, 6\n" \
  "s_lshr_b32 s46, s36, 2\n" \
  "s_lshl_b32 s47, s46, 3\n" \
  "s_add_u32 s47, s47, 0x18\n" \
  "s_load_dwordx2 s[16:17], s[0:1], s47\n" \
  "s_load_dwordx2 s[24:25], s[0:1], 0x28\n" \
  "s_load_dwordx2 s[28:29], s[0:1], 0x30\n" \
  "s_and_b32 s37, s2, 7\n" \
  "s_lshr_b32 s38, s2, 3\n" \
  "v_lshrrev_b32 v80, 3, v250\n" \
  "v_lshrrev_b32 v81, 4, v250\n" \
  "v_lshlrev_b32 v251, 4, v250\n" \
  "s_lshr_b32 s48, s2, 6\n" \
  "s_lshr_b32 s42, s37, 1\n" \
  "s_lshl2_add_u32 s42, s42, s48\n" \
  "s_and_b32 s49, s38, 7\n" \
  "s_and_b32 s41, s37, 1\n" \
  "s_lshl3_add_u32 s41, s41, s49\n" \
  "s_lshl3_add_u32 s47, s42, s36\n" \
  "s_lshl_b32 s47, s47, 14\n" \
  "v_xor_b32 v82, v250, v81\n" \
  "v_and_b32 v82, 7, v82\n" \
  "v_lshl_add_u32 v82, v82, 4, s47\n" \
  "v_lshl_add_u32 v1, v80, 10, v82\n" \
  "v_xor_b32 v2, 64, v1\n" \
  "v_add_u32 v2, 7168, v2\n" \
    \
  "s_add_u32 s51, s48, s49\n" \
  "s_and_b32 s51, s51, 1\n" \
  "s_lshl_b32 s51, s51, 8\n" \
  "s_lshr_b32 s50, s37, 1\n" \
  "s_xor_b32 s50, s50, s37\n" \
  "s_and_b32 s50, s50, 1\n" \
  "s_lshl_b32 s50, s50, 9\n" \
  "s_xor_b32 s51, s51, s50\n" \
  "s_and_b32 s50, s37, 4\n" \
  "s_lshl_b32 s50, s50, 5\n" \
  "s_xor_b32 s51, s51, s50\n" \
  "s_lshr_b32 s44, s41, 1\n" \
  "s_and_b32 s43, s41, 1\n" \
  "v_lshlrev_b32 v83, 2, v250\n" \
  "v_and_b32 v84, 64, v83\n" \
  "v_and_b32 v85, 0x70, v251\n" \
  "v_xor_b32 v84, v84, v85\n" \
  "s_lshl3_add_u32 s48, s43, s44\n" \
  "s_lshl_b32 s48, s48, 7\n" \
  "s_mul_i32 s49, s36, 24576\n" \
  "s_add_u32 s48, s48, s49\n" \
  "s_add_u32 s48, s48, 1024\n" \
  "v_mul_u32_u24 v3, 3072, v80\n" \
  "v_add3_u32 v3, v3, v84, s48\n" \
  "s_mov_b32 s6, 0x200000\n" \
  "s_mov_b32 s7, 0x20000\n" \
  "s_mov_b32 s10, 0x180000\n" \
  "s_mov_b32 s11, 0x20000\n" \
  "s_lshl_b32 s53, s36, 11\n" \
  "s_lshl_b32 s54, s36, 10\n" \
  "s_add_u32 s54, s54, 65536\n" \
  "s_add_u32 m0, s53, 0\n" \
  "s_xor_b32 s50, s51, 0\n" \
  "s_waitcnt lgkmcnt(0)\n" \
  "buffer_load_dwordx4 v1, s[4:7], s50 offen lds\n" \
  "buffer_load_dwordx4 v2, s[4:7], s50 offen offset:1024 lds\n" \
  "s_add_u32 m0, s54, 0\n" \
  "s_mul_i32 s50, s50, 1536\n" \
  "buffer_load_dwordx4 v3, s[8:11], s50 offen lds\n" \
  "s_mov_b32 s14, 0x1000000\n" \
  "s_mov_b32 s15, 0x20000\n" \
  "s_lshl_b32 s47, s2, 8\n" \
  "s_add_u32 s47, s47, 0xf00000\n" \
  "v_add_u32 v252, s47, v83\n" \
    \
  "s_and_b32 s45, s36, 3\n" \
  "s_lshl_b32 s49, s46, 1\n" \
  "s_xor_b32 s49, s49, s45\n" \
  "v_and_b32 v86, 31, v250\n" \
  "v_lshrrev_b32 v87, 5, v250\n" \
  "v_bfe_u32 v88, v250, 2, 2\n" \
  "v_and_b32 v90, 3, v250\n" \
  "v_and_b32 v89, 16, v250\n" \
  "v_lshlrev_b32 v89, 1, v89\n" \
  "v_lshl_or_b32 v89, v90, 3, v89\n" \
  "v_bfe_u32 v91, v250, 1, 3\n" \
  "v_xor_b32 v91, v91, v87\n" \
  "v_lshlrev_b32 v91, 4, v91\n" \
  "v_lshl_or_b32 v91, v86, 7, v91\n" \
  "s_lshl_b32 s47, s45, 12\n" \
  "s_lshl_b32 s48, s49, 5\n" \
  "s_or_b32 s47, s47, s48\n" \
  "v_xor_b32 v4, s47, v91\n" \
  "v_xor_b32 v5, 32, v4\n" \
  "v_xor_b32 v6, 64, v4\n" \
  "v_xor_b32 v7, 96, v4\n" \
  "v_lshl_or_b32 v91, v87, 3, v88\n" \
  "v_lshl_or_b32 v92, v91, 6, v89\n" \
  "v_and_b32 v94, 8, v250\n" \
  "v_lshl_or_b32 v94, v94, 3, v89\n" \
  "v_lshl_or_b32 v91, v91, 7, v94\n" \
  "s_lshl_b32 s47, s49, 11\n" \
  "s_lshl_b32 s48, s46, 6\n" \
  "s_or_b32 s47, s47, s48\n" \
  "s_or_b32 s47, s47, 65536\n" \
  "v_xor_b32 v8, s47, v91\n" \
  "v_xor_b32 v9, 2048, v8\n" \
  "v_xor_b32 v10, 4096, v8\n" \
  "v_xor_b32 v11, 6144, v8\n" \
  "s_add_u32 m0, s53, 16384\n" \
  "s_xor_b32 s50, s51, 128\n" \
  "buffer_load_dwordx4 v1, s[4:7], s50 offen lds\n" \
  "buffer_load_dwordx4 v2, s[4:7], s50 offen offset:1024 lds\n" \
  "s_add_u32 m0, s54, 8192\n" \
  "s_mul_i32 s50, s50, 1536\n" \
  "buffer_load_dwordx4 v3, s[8:11], s50 offen lds\n" \
    \
  "s_getpc_b64 s[32:33]\n" \
  "fbk_code_begin:\n" \
  "s_mov_b32 s34, fbk_code_end-fbk_code_begin\n" \
  "s_mov_b32 s35, 0x20000\n" \
  "s_lshl3_add_u32 s57, s38, s36\n" \
  "s_lshl_b32 s57, s57, 7\n" \
  "s_mov_b32 m0, 153600\n" \
  "buffer_load_dword v24, off, s[32:35], s57\n" \
  "s_cmp_lg_u32 s36, 7\n" \
  "s_cbranch_scc1 fbk_a2\n" \
    \
  "buffer_load_dword v252, s[12:15], 0 offen sc1 lds\n" \
  "fbk_a2:\n" \
  "s_lshl_b32 s47, s49, 10\n" \
  "s_or_b32 s47, s47, 98304\n" \
  "v_or_b32 v12, s47, v92\n" \
  "v_xor_b32 v13, 1024, v12\n" \
  "v_xor_b32 v14, 2048, v12\n" \
  "v_xor_b32 v15, 3072, v12\n" \
  "v_mov_b32 v27, 0\n" \
  "v_mov_b32 v28, 0\n" \
  "v_mov_b32 v29, 0\n" \
  "v_mov_b32 v30, 0\n" \
  "v_mov_b32 v31, 0\n" \
  "s_and_b32 s39, s38, 7\n" \
  "s_lshr_b32 s40, s2, 6\n" \
  "v_lshrrev_b32 v0, 2, v250\n" \
  "v_mul_u32_u24 v0, 3072, v0\n" \
  "v_lshl_add_u32 v0, v90, 4, v0\n" \
  "s_lshr_b32 s52, s51, 7\n" \
  "s_xor_b32 s52, s52, s36\n" \
  "s_mul_i32 s52, s52, 196608\n" \
  "s_lshl_b32 s48, s41, 6\n" \
  "s_add_u32 s52, s52, s48\n" \
    \
  "s_cmp_eq_u32 s44, 3\n" \
  "s_cselect_b32 s48, s40, 0\n" \
  "s_lshl2_add_u32 s87, s36, s48\n" \
  "s_lshl_b32 s87, s87, 10\n" \
  "s_add_u32 s87, s87, 98304\n" \
  "s_xor_b32 s47, s48, 0\n" \
  "s_mul_i32 s47, s47, 49152\n" \
  "s_add_u32 s83, s47, s52\n" \
  "s_xor_b32 s88, s87, 1024\n" \
  "s_xor_b32 s47, s48, 1\n" \
  "s_mul_i32 s47, s47, 49152\n" \
  "s_add_u32 s84, s47, s52\n" \
  "s_xor_b32 s89, s87, 2048\n" \
  "s_xor_b32 s47, s48, 2\n" \
  "s_mul_i32 s47, s47, 49152\n" \
  "s_add_u32 s85, s47, s52\n" \
  "s_xor_b32 s90, s87, 3072\n" \
  "s_xor_b32 s47, s48, 3\n" \
  "s_mul_i32 s47, s47, 49152\n" \
  "s_add_u32 s86, s47, s52\n" \
  "s_lshl_b32 s48, s36, 9\n" \
  "s_or_b32 s48, s48, 131072\n" \
  "v_lshl_or_b32 v16, v250, 3, s48\n" \
  "v_xor_b32 v17, 2048, v16\n" \
  "s_lshl_b32 s48, s46, 10\n" \
  "s_or_b32 s48, s48, 135168\n" \
  "v_or_b32 v19, s48, v251\n" \
  "v_lshl_or_b32 v18, s45, 2, v19\n" \
  "s_mul_i32 s48, s45, s46\n" \
  "s_lshl_b32 s48, s48, 2\n" \
  "s_or_b32 s48, s48, 139264\n" \
  "v_or_b32 v25, s48, v251\n" \
  "s_lshl_b32 s48, s45, 10\n" \
  "s_or_b32 s48, s48, 155648\n" \
  "v_or_b32 v26, s48, v251\n" \
  "v_or_b32 v253, 153600, v83\n" \
  "s_lshl_b32 s48, s39, 7\n" \
  "v_lshl_or_b32 v254, v80, 10, v84\n" \
  "v_or_b32 v254, s48, v254\n" \
  "v_and_b32 v233, 0xf0, v251\n" \
  "v_lshl_or_b32 v233, v87, 8, v233\n" \
  "v_or_b32 v233, 1024, v233\n" \
  "s_lshl1_add_u32 s55, s44, s46\n" \
  "s_lshl_b32 s55, s55, 7\n" \
  "s_lshl2_add_u32 s48, s42, s45\n" \
  "s_add_u32 s55, s55, s48\n" \
  "s_lshl_b32 s55, s55, 11\n" \
    \
  "s_cmp_lg_u32 s45, 3\n" \
  "s_cbranch_scc1 fbk_a11\n" \
  "v_lshrrev_b32 v228, 3, v250\n" \
    \
  "v_and_b32 v225, 1, v250\n" \
  "v_bfe_u32 v226, v250, 1, 2\n" \
  "v_lshl_or_b32 v225, v226, 3, v225\n" \
  "v_lshlrev_b32 v225, 11, v225\n" \
  "v_lshl_or_b32 v225, v228, 9, v225\n" \
  "v_and_b32 v227, 3, v250\n" \
  "v_bfe_u32 v226, v250, 2, 1\n" \
  "s_and_b32 s48, s2, 0x31\n" \
  "s_lshl_b32 s48, s48, 6\n" \
  "s_lshl1_add_u32 s49, s42, s43\n" \
  "s_add_u32 s48, s48, s49\n" \
  "s_lshl2_add_u32 s48, s48, 0xf00000\n" \
  "v_add_u32 v249, s48, v225\n" \
  "v_lshlrev_b32 v226, 8, v226\n" \
  "v_lshl_or_b32 v226, v227, 12, v226\n" \
  "s_and_b32 s48, s2, 0xce\n" \
  "s_lshl_b32 s48, s48, 6\n" \
  "s_add_u32 s48, s48, s44\n" \
  "s_lshl2_add_u32 s48, s48, 0xf00080\n" \
  "v_add_u32 v226, s48, v226\n" \
  "v_cmp_gt_u32 vcc, 32, v250\n" \
    \
  "s_and_b32 s48, s42, 1\n" \
  "s_lshl_b32 s48, s48, 7\n" \
  "v_cndmask_b32 v249, v226, v249, vcc\n" \
  "s_lshr_b32 s49, s42, 1\n" \
  "s_add_u32 s48, s48, s49\n" \
  "s_lshl_b32 s48, s48, 8\n" \
  "s_add_u32 s48, s48, 0xf000a0\n" \
  "v_lshl_add_u32 v235, v250, 11, s48\n" \
  "v_cndmask_b32 v235, v226, v235, vcc\n" \
  "fbk_a11:\n" \
    \
  "v_and_b32 v224, 31, v250\n" \
  "v_lshlrev_b32 v228, 2, v250\n" \
  "v_cmp_eq_u32 vcc, 16, v224\n" \
  "v_mov_b32 v248, 0x3f803f80\n" \
  "v_mul_u32_u24 v225, 43, v250\n" \
  "v_cndmask_b32 v248, 0, v248, vcc\n" \
  "v_lshrrev_b32 v225, 8, v225\n" \
  "v_mul_u32_u24 v226, 96, v225\n" \
  "v_sub_u32 v226, v251, v226\n" \
  "s_lshl_b32 s48, s46, 2\n" \
  "v_xor_b32 v225, s48, v225\n" \
  "v_mul_u32_u24 v225, 2752, v225\n" \
  "v_add_u32 v234, v225, v226\n" \
  "s_mul_i32 s58, s36, 12288\n" \
  "s_and_b32 s48, s38, 3\n" \
  "s_xor_b32 s48, s48, s45\n" \
  "s_mul_i32 s59, s48, 352256\n" \
  "s_lshr_b32 s48, s36, 1\n" \
  "s_xor_b32 s48, s48, s37\n" \
  "s_and_b32 s48, s48, 1\n" \
  "s_lshl1_add_u32 s48, s48, s46\n" \
  "s_and_b32 s60, s36, 1\n" \
  "s_lshl1_add_u32 s60, s48, s60\n" \
  "s_lshl_b32 s60, s60, 16\n" \
  "s_mul_i32 s48, s38, 96\n" \
  "s_add_u32 s59, s59, s48\n" \
  "s_lshr_b32 s91, s2, 5\n" \
  "s_and_b32 s91, s91, 4\n" \
  "s_xor_b32 s91, s91, s36\n" \
  "s_lshl2_add_u32 s82, s37, s40\n" \
  "s_lshl3_add_u32 s82, s82, s91\n" \
  "s_lshl_b32 s82, s82, 6\n" \
  "s_add_u32 s82, s82, s39\n" \
  "s_lshl_b32 s82, s82, 7\n" \
  "s_cmp_lt_u32 s38, 29\n" \
  "s_cselect_b32 s18, 1409024, 0\n" \
  "s_cselect_b32 s66, 0, 8\n" \
  "s_add_u32 s66, s66, s91\n" \
  "s_mov_b32 s19, 0x20000\n" \
  "s_mov_b32 s22, 0x80000\n" \
  "s_mov_b32 s23, 0x20000\n" \
  "s_mov_b32 s26, 1409024\n" \
  "s_mov_b32 s27, 0x20000\n" \
  "s_mov_b32 s30, 0x200000\n" \
  "s_mov_b32 s31, 0x20000\n" \
  "s_mov_b32 s76, 0xffff\n" \
  "s_mov_b32 s77, 0xffff\n" \
  "s_and_b32 s49, s36, 1\n" \
  "s_lshr_b32 s50, s36, 1\n" \
  "s_and_b32 s48, s50, 1\n" \
  "s_lshl1_add_u32 s73, s48, s49\n" \
  "s_mul_i32 s48, s48, 84\n" \
  "s_add_u32 s48, s48, s49\n" \
  "s_lshr_b32 s47, s2, 7\n" \
  "s_mul_i32 s47, s47, 6\n" \
  "s_add_u32 s74, s48, s47\n" \
  "s_sub_u32 s62, s48, s47\n" \
  "s_add_u32 s62, s62, 6\n" \
  "s_add_u32 s73, s73, 168\n" \
  "s_add_u32 s47, s62, 0\n" \
  "s_add_u32 s48, s74, 60\n" \
  "s_cmp_lt_u32 s91, 4\n" \
  "s_cselect_b32 s72, s74, s47\n" \
  "s_cselect_b32 s73, s48, s73\n" \
  "s_lshl_b32 s72, s72, 13\n" \
  "s_lshl_b32 s73, s73, 13\n" \
  "s_lshl_b32 s74, s74, 13\n" \
  "s_lshl_b32 s62, s62, 13\n" \
  "s_lshl1_add_u32 s71, s41, s49\n" \
  "s_and_b32 s48, s42, 12\n" \
  "s_lshl4_add_u32 s56, s71, s48\n" \
  "s_add_u32 s56, s56, s50\n" \
  "s_lshl_b32 s56, s56, 13\n" \
  "s_lshl_b32 s48, s48, 1\n" \
  "s_lshl_b32 s81, 0xff, s48\n" \
  "s_lshl2_add_u32 s61, s42, s50\n" \
  "s_lshl_b32 s61, s61, 5\n" \
  "s_add_u32 s61, s61, s71\n" \
  "s_lshl_b32 s61, s61, 10\n" \
  "s_add_u32 s61, s61, 0x400000\n" \
  "v_or_b32 v225, 155648, v251\n" \
  "v_or_b32 v226, 98304, v251\n" \
  "v_or_b32 v227, 147456, v228\n" \
  "s_add_u32 s48, s50, 1\n" \
  "s_and_b32 s48, s48, 3\n" \
  "s_lshl2_add_u32 s48, s49, s48\n" \
  "v_lshl_add_u32 v244, s48, 10, v225\n" \
  "s_mul_i32 s48, s48, 3\n" \
  "v_lshl_add_u32 v236, s48, 11, v226\n" \
  "v_lshl_add_u32 v239, s48, 8, v227\n" \
  "s_add_u32 s48, s50, 2\n" \
  "s_and_b32 s48, s48, 3\n" \
  "s_lshl2_add_u32 s48, s49, s48\n" \
  "v_lshl_add_u32 v245, s48, 10, v225\n" \
  "s_mul_i32 s48, s48, 3\n" \
  "s_add_u32 s48, s48, 1\n" \
  "v_lshl_add_u32 v237, s48, 11, v226\n" \
  "v_lshl_add_u32 v240, s48, 8, v227\n" \
  "s_add_u32 s48, s50, 3\n" \
  "s_and_b32 s48, s48, 3\n" \
  "s_lshl2_add_u32 s48, s49, s48\n" \
  "v_lshl_add_u32 v246, s48, 10, v225\n" \
  "s_mul_i32 s48, s48, 3\n" \
  "s_add_u32 s48, s48, 2\n" \
  "v_lshl_add_u32 v238, s48, 11, v226\n" \
  "v_lshl_add_u32 v241, s48, 8, v227\n" \
  "s_add_u32 s48, s50, 4\n" \
  "s_and_b32 s48, s48, 3\n" \
  "s_lshl2_add_u32 s48, s49, s48\n" \
  "v_lshl_add_u32 v247, s48, 10, v225\n" \
  "s_mul_i32 s48, s48, 3\n" \
  "v_lshl_add_u32 v242, s48, 11, v226\n" \
  "v_lshl_add_u32 v243, s48, 8, v227\n" \
  "s_add_u32 m0, s53, 32768\n" \
  "s_xor_b32 s50, s51, 256\n" \
  "buffer_load_dwordx4 v1, s[4:7], s50 offen lds\n" \
  "buffer_load_dwordx4 v2, s[4:7], s50 offen offset:1024 lds\n" \
  "s_add_u32 m0, s54, 16384\n" \
  "s_mul_i32 s50, s50, 1536\n" \
  "buffer_load_dwordx4 v3, s[8:11], s50 offen lds\n" \
  "s_waitcnt vmcnt(7) lgkmcnt(0)\n" \
  "s_barrier\n" \
  "ds_read_b128 v[96:99], v4 offset:0\n" \
  "ds_read_b64_tr_b16 v[48:49], v8 offset:0\n" \
  "ds_read_b64_tr_b16 v[50:51], v8 offset:512\n" \
  "ds_read_b128 v[100:103], v5 offset:0\n" \
  "ds_read_b64_tr_b16 v[52:53], v9 offset:0\n" \
  "ds_read_b64_tr_b16 v[54:55], v9 offset:512\n" \
  "ds_read_b128 v[104:107], v6 offset:0\n" \
  "ds_read_b64_tr_b16 v[56:57], v10 offset:0\n" \
  "ds_read_b64_tr_b16 v[58:59], v10 offset:512\n" \
  "ds_read_b128 v[108:111], v7 offset:0\n" \
  "ds_read_b64_tr_b16 v[60:61], v11 offset:0\n" \
  "ds_read_b64_tr_b16 v[62:63], v11 offset:512\n" \
  "s_add_u32 m0, s53, 49152\n" \
  "s_xor_b32 s50, s51, 384\n" \
  "buffer_load_dwordx4 v1, s[4:7], s50 offen lds\n" \
  "buffer_load_dwordx4 v2, s[4:7], s50 offen offset:1024 lds\n" \
  "s_add_u32 m0, s54, 24576\n" \
  "s_mul_i32 s50, s50, 1536\n" \
  "buffer_load_dwordx4 v3, s[8:11], s50 offen lds\n" \
  "s_cmp_lg_u32 s43, 0\n" \
  "s_cbranch_scc1 fbk_av\n" \
    \
  "v_and_b32 v20, 19, v250\n" \
  "v_and_b32 v21, 4, v250\n" \
  "v_lshl_or_b32 v20, v21, 1, v20\n" \
  "v_bfe_u32 v21, v250, 3, 1\n" \
  "v_lshl_or_b32 v20, v21, 2, v20\n" \
  "v_lshlrev_b32 v20, 4, v20\n" \
  "v_lshl_or_b32 v20, v87, 9, v20\n" \
  "v_add_u32 v21, 131072, v20\n" \
    \
  "s_cmp_ge_u32 s36, 4\n" \
  "s_cbranch_scc1 fbk_sk0a\n" \
  "s_waitcnt vmcnt(6) lgkmcnt(0)\n" \
  "s_barrier\n" \
  "fbk_sk0a:\n" \
  "s_waitcnt lgkmcnt(8)\n" \
  "v_dot2c_f32_bf16 v29, 0x3f803f80, v96\n" \
  "v_dot2c_f32_bf16 v30, v96, v96\n" \
  "v_dot2c_f32_bf16 v31, 0x3f803f80, v48\n" \
  "v_dot2c_f32_bf16 v27, 0x3f803f80, v100\n" \
  "v_dot2c_f32_bf16 v28, v100, v100\n" \
  "v_dot2c_f32_bf16 v29, 0x3f803f80, v97\n" \
  "v_dot2c_f32_bf16 v30, v97, v97\n" \
  "v_dot2c_f32_bf16 v31, 0x3f803f80, v49\n" \
  "v_dot2c_f32_bf16 v27, 0x3f803f80, v101\n" \
  "v_dot2c_f32_bf16 v28, v101, v101\n" \
  "v_dot2c_f32_bf16 v29, 0x3f803f80, v98\n" \
  "v_dot2c_f32_bf16 v30, v98, v98\n" \
  "v_dot2c_f32_bf16 v31, 0x3f803f80, v50\n" \
  "v_dot2c_f32_bf16 v27, 0x3f803f80, v102\n" \
  "v_dot2c_f32_bf16 v28, v102, v102\n" \
  "v_dot2c_f32_bf16 v29, 0x3f803f80, v99\n" \
  "v_dot2c_f32_bf16 v30, v99, v99\n" \
  "v_dot2c_f32_bf16 v31, 0x3f803f80, v51\n" \
  "v_dot2c_f32_bf16 v27, 0x3f803f80, v103\n" \
  "v_dot2c_f32_bf16 v28, v103, v103\n" \
  "s_waitcnt lgkmcnt(0)\n" \
  "v_mfma_f32_32x32x16_bf16 v[32:47], v[48:51], v[96:99], 0\n" \
  "v_mfma_f32_32x32x16_bf16 v[32:47], v[52:55], v[100:103], v[32:47]\n" \
  "v_mfma_f32_32x32x16_bf16 v[32:47], v[56:59], v[104:107], v[32:47]\n" \
  "v_mfma_f32_32x32x16_bf16 v[32:47], v[60:63], v[108:111], v[32:47]\n" \
  "s_cmp_lt_u32 s36, 4\n" \
  "s_cbranch_scc1 fbk_sk0b\n" \
  "s_waitcnt vmcnt(6)\n" \
  "s_barrier\n" \
  "fbk_sk0b:\n" \
  "s_add_u32 m0, s53, 0\n" \
  "s_xor_b32 s50, s51, 512\n" \
  "buffer_load_dwordx4 v1, s[4:7], s50 offen lds\n" \
  "ds_read_b128 v[112:115], v4 offset:16384\n" \
  "ds_read_b64_tr_b16 v[64:65], v8 offset:8192\n" \
  "ds_read_b64_tr_b16 v[66:67], v8 offset:8704\n" \
  "ds_read_b128 v[116:119], v5 offset:16384\n" \
  "ds_read_b64_tr_b16 v[68:69], v9 offset:8192\n" \
  "ds_read_b64_tr_b16 v[70:71], v9 offset:8704\n" \
  "buffer_load_dwordx4 v2, s[4:7], s50 offen offset:1024 lds\n" \
  "ds_read_b128 v[120:123], v6 offset:16384\n" \
  "ds_read_b64_tr_b16 v[72:73], v10 offset:8192\n" \
  "ds_read_b64_tr_b16 v[74:75], v10 offset:8704\n" \
  "ds_read_b128 v[124:127], v7 offset:16384\n" \
  "ds_read_b64_tr_b16 v[76:77], v11 offset:8192\n" \
  "ds_read_b64_tr_b16 v[78:79], v11 offset:8704\n" \
  "s_add_u32 m0, s54, 0\n" \
  "s_mul_i32 s50, s50, 1536\n" \
  "buffer_load_dwordx4 v3, s[8:11], s50 offen lds\n" \
    \
  "s_cmp_ge_u32 s36, 4\n" \
  "s_cbranch_scc1 fbk_sk1a\n" \
  "s_waitcnt vmcnt(6) lgkmcnt(0)\n" \
  "s_barrier\n" \
  "fbk_sk1a:\n" \
  "s_waitcnt lgkmcnt(8)\n" \
  "v_dot2c_f32_bf16 v29, 0x3f803f80, v112\n" \
  "v_dot2c_f32_bf16 v30, v112, v112\n" \
  "v_dot2c_f32_bf16 v31, 0x3f803f80, v64\n" \
  "v_dot2c_f32_bf16 v27, 0x3f803f80, v116\n" \
  "v_dot2c_f32_bf16 v28, v116, v116\n" \
  "v_dot2c_f32_bf16 v29, 0x3f803f80, v113\n" \
  "v_dot2c_f32_bf16 v30, v113, v113\n" \
  "v_dot2c_f32_bf16 v31, 0x3f803f80, v65\n" \
  "v_dot2c_f32_bf16 v27, 0x3f803f80, v117\n" \
  "v_dot2c_f32_bf16 v28, v117, v117\n" \
  "v_dot2c_f32_bf16 v29, 0x3f803f80, v114\n" \
  "v_dot2c_f32_bf16 v30, v114, v114\n" \
  "v_dot2c_f32_bf16 v31, 0x3f803f80, v66\n" \
  "v_dot2c_f32_bf16 v27, 0x3f803f80, v118\n" \
  "v_dot2c_f32_bf16 v28, v118, v118\n" \
  "v_dot2c_f32_bf16 v29, 0x3f803f80, v115\n" \
  "v_dot2c_f32_bf16 v30, v115, v115\n" \
  "v_dot2c_f32_bf16 v31, 0x3f803f80, v67\n" \
  "v_dot2c_f32_bf16 v27, 0x3f803f80, v119\n" \
  "v_dot2c_f32_bf16 v28, v119, v119\n" \
  "s_waitcnt lgkmcnt(0)\n" \
  "v_mfma_f32_32x32x16_bf16 v[32:47], v[64:67], v[112:115], v[32:47]\n" \
  "v_mfma_f32_32x32x16_bf16 v[32:47], v[68:71], v[116:119], v[32:47]\n" \
  "v_mfma_f32_32x32x16_bf16 v[32:47], v[72:75], v[120:123], v[32:47]\n" \
  "v_mfma_f32_32x32x16_bf16 v[32:47], v[76:79], v[124:127], v[32:47]\n" \
  "s_cmp_lt_u32 s36, 4\n" \
  "s_cbranch_scc1 fbk_sk1b\n" \
  "s_waitcnt vmcnt(6)\n" \
  "s_barrier\n" \
  "fbk_sk1b:\n" \
  "s_add_u32 m0, s53, 16384\n" \
  "s_xor_b32 s50, s51, 640\n" \
  "buffer_load_dwordx4 v1, s[4:7], s50 offen lds\n" \
  "ds_read_b128 v[128:131], v4 offset:32768\n" \
  "ds_read_b64_tr_b16 v[48:49], v8 offset:16384\n" \
  "ds_read_b64_tr_b16 v[50:51], v8 offset:16896\n" \
  "ds_read_b128 v[132:135], v5 offset:32768\n" \
  "ds_read_b64_tr_b16 v[52:53], v9 offset:16384\n" \
  "ds_read_b64_tr_b16 v[54:55], v9 offset:16896\n" \
  "buffer_load_dwordx4 v2, s[4:7], s50 offen offset:1024 lds\n" \
  "ds_read_b128 v[136:139], v6 offset:32768\n" \
  "ds_read_b64_tr_b16 v[56:57], v10 offset:16384\n" \
  "ds_read_b64_tr_b16 v[58:59], v10 offset:16896\n" \
  "ds_read_b128 v[140:143], v7 offset:32768\n" \
  "ds_read_b64_tr_b16 v[60:61], v11 offset:16384\n" \
  "ds_read_b64_tr_b16 v[62:63], v11 offset:16896\n" \
  "s_add_u32 m0, s54, 8192\n" \
  "s_mul_i32 s50, s50, 1536\n" \
  "buffer_load_dwordx4 v3, s[8:11], s50 offen lds\n" \
    \
  "s_cmp_ge_u32 s36, 4\n" \
  "s_cbranch_scc1 fbk_sk2a\n" \
  "s_waitcnt vmcnt(6) lgkmcnt(0)\n" \
  "s_barrier\n" \
  "fbk_sk2a:\n" \
  "s_waitcnt lgkmcnt(8)\n" \
  "v_dot2c_f32_bf16 v29, 0x3f803f80, v128\n" \
  "v_dot2c_f32_bf16 v30, v128, v128\n" \
  "v_dot2c_f32_bf16 v31, 0x3f803f80, v48\n" \
  "v_dot2c_f32_bf16 v27, 0x3f803f80, v132\n" \
  "v_dot2c_f32_bf16 v28, v132, v132\n" \
  "v_dot2c_f32_bf16 v29, 0x3f803f80, v129\n" \
  "v_dot2c_f32_bf16 v30, v129, v129\n" \
  "v_dot2c_f32_bf16 v31, 0x3f803f80, v49\n" \
  "v_dot2c_f32_bf16 v27, 0x3f803f80, v133\n" \
  "v_dot2c_f32_bf16 v28, v133, v133\n" \
  "v_dot2c_f32_bf16 v29, 0x3f803f80, v130\n" \
  "v_dot2c_f32_bf16 v30, v130, v130\n" \
  "v_dot2c_f32_bf16 v31, 0x3f803f80, v50\n" \
  "v_dot2c_f32_bf16 v27, 0x3f803f80, v134\n" \
  "v_dot2c_f32_bf16 v28, v134, v134\n" \
  "v_dot2c_f32_bf16 v29, 0x3f803f80, v131\n" \
  "v_dot2c_f32_bf16 v30, v131, v131\n" \
  "v_dot2c_f32_bf16 v31, 0x3f803f80, v51\n" \
  "v_dot2c_f32_bf16 v27, 0x3f803f80, v135\n" \
  "v_dot2c_f32_bf16 v28, v135, v135\n" \
  "s_waitcnt lgkmcnt(0)\n" \
  "v_mfma_f32_32x32x16_bf16 v[32:47], v[48:51], v[128:131], v[32:47]\n" \
  "v_mfma_f32_32x32x16_bf16 v[32:47], v[52:55], v[132:135], v[32:47]\n" \
  "v_mfma_f32_32x32x16_bf16 v[32:47], v[56:59], v[136:139], v[32:47]\n" \
  "v_mfma_f32_32x32x16_bf16 v[32:47], v[60:63], v[140:143], v[32:47]\n" \
  "s_cmp_lt_u32 s36, 4\n" \
  "s_cbranch_scc1 fbk_sk2b\n" \
  "s_waitcnt vmcnt(6)\n" \
  "s_barrier\n" \
  "fbk_sk2b:\n" \
  "s_add_u32 m0, s53, 32768\n" \
  "s_xor_b32 s50, s51, 768\n" \
  "buffer_load_dwordx4 v1, s[4:7], s50 offen lds\n" \
  "ds_read_b128 v[144:147], v4 offset:49152\n" \
  "ds_read_b64_tr_b16 v[64:65], v8 offset:24576\n" \
  "ds_read_b64_tr_b16 v[66:67], v8 offset:25088\n" \
  "ds_read_b128 v[148:151], v5 offset:49152\n" \
  "ds_read_b64_tr_b16 v[68:69], v9 offset:24576\n" \
  "ds_read_b64_tr_b16 v[70:71], v9 offset:25088\n" \
  "buffer_load_dwordx4 v2, s[4:7], s50 offen offset:1024 lds\n" \
  "ds_read_b128 v[152:155], v6 offset:49152\n" \
  "ds_read_b64_tr_b16 v[72:73], v10 offset:24576\n" \
  "ds_read_b64_tr_b16 v[74:75], v10 offset:25088\n" \
  "ds_read_b128 v[156:159], v7 offset:49152\n" \
  "ds_read_b64_tr_b16 v[76:77], v11 offset:24576\n" \
  "ds_read_b64_tr_b16 v[78:79], v11 offset:25088\n" \
  "s_add_u32 m0, s54, 16384\n" \
  "s_mul_i32 s50, s50, 1536\n" \
  "buffer_load_dwordx4 v3, s[8:11], s50 offen lds\n" \
    \
  "s_cmp_ge_u32 s36, 4\n" \
  "s_cbranch_scc1 fbk_sk3a\n" \
  "s_waitcnt vmcnt(6) lgkmcnt(0)\n" \
  "s_barrier\n" \
  "fbk_sk3a:\n" \
  "s_waitcnt lgkmcnt(8)\n" \
  "v_dot2c_f32_bf16 v29, 0x3f803f80, v144\n" \
  "v_dot2c_f32_bf16 v30, v144, v144\n" \
  "v_dot2c_f32_bf16 v31, 0x3f803f80, v64\n" \
  "v_dot2c_f32_bf16 v27, 0x3f803f80, v148\n" \
  "v_dot2c_f32_bf16 v28, v148, v148\n" \
  "v_dot2c_f32_bf16 v29, 0x3f803f80, v145\n" \
  "v_dot2c_f32_bf16 v30, v145, v145\n" \
  "v_dot2c_f32_bf16 v31, 0x3f803f80, v65\n" \
  "v_dot2c_f32_bf16 v27, 0x3f803f80, v149\n" \
  "v_dot2c_f32_bf16 v28, v149, v149\n" \
  "v_dot2c_f32_bf16 v29, 0x3f803f80, v146\n" \
  "v_dot2c_f32_bf16 v30, v146, v146\n" \
  "v_dot2c_f32_bf16 v31, 0x3f803f80, v66\n" \
  "v_dot2c_f32_bf16 v27, 0x3f803f80, v150\n" \
  "v_dot2c_f32_bf16 v28, v150, v150\n" \
  "v_dot2c_f32_bf16 v29, 0x3f803f80, v147\n" \
  "v_dot2c_f32_bf16 v30, v147, v147\n" \
  "v_dot2c_f32_bf16 v31, 0x3f803f80, v67\n" \
  "v_dot2c_f32_bf16 v27, 0x3f803f80, v151\n" \
  "v_dot2c_f32_bf16 v28, v151, v151\n" \
  "s_waitcnt lgkmcnt(0)\n" \
  "v_mfma_f32_32x32x16_bf16 v[32:47], v[64:67], v[144:147], v[32:47]\n" \
  "v_mfma_f32_32x32x16_bf16 v[32:47], v[68:71], v[148:151], v[32:47]\n" \
  "v_mfma_f32_32x32x16_bf16 v[32:47], v[72:75], v[152:155], v[32:47]\n" \
  "v_mfma_f32_32x32x16_bf16 v[32:47], v[76:79], v[156:159], v[32:47]\n" \
  "s_cmp_lt_u32 s36, 4\n" \
  "s_cbranch_scc1 fbk_sk3b\n" \
  "s_waitcnt vmcnt(6)\n" \
  "s_barrier\n" \
  "fbk_sk3b:\n" \
  "s_add_u32 m0, s53, 49152\n" \
  "s_xor_b32 s50, s51, 896\n" \
  "buffer_load_dwordx4 v1, s[4:7], s50 offen lds\n" \
  "ds_read_b128 v[160:163], v4 offset:0\n" \
  "ds_read_b64_tr_b16 v[48:49], v8 offset:0\n" \
  "ds_read_b64_tr_b16 v[50:51], v8 offset:512\n" \
  "ds_read_b128 v[164:167], v5 offset:0\n" \
  "ds_read_b64_tr_b16 v[52:53], v9 offset:0\n" \
  "ds_read_b64_tr_b16 v[54:55], v9 offset:512\n" \
  "buffer_load_dwordx4 v2, s[4:7], s50 offen offset:1024 lds\n" \
  "ds_read_b128 v[168:171], v6 offset:0\n" \
  "ds_read_b64_tr_b16 v[56:57], v10 offset:0\n" \
  "ds_read_b64_tr_b16 v[58:59], v10 offset:512\n" \
  "ds_read_b128 v[172:175], v7 offset:0\n" \
  "ds_read_b64_tr_b16 v[60:61], v11 offset:0\n" \
  "ds_read_b64_tr_b16 v[62:63], v11 offset:512\n" \
  "s_add_u32 m0, s54, 24576\n" \
  "s_mul_i32 s50, s50, 1536\n" \
  "buffer_load_dwordx4 v3, s[8:11], s50 offen lds\n" \
    \
  "s_cmp_ge_u32 s36, 4\n" \
  "s_cbranch_scc1 fbk_sk4a\n" \
  "s_waitcnt vmcnt(6) lgkmcnt(0)\n" \
  "s_barrier\n" \
  "fbk_sk4a:\n" \
  "s_waitcnt lgkmcnt(8)\n" \
  "v_dot2c_f32_bf16 v29, 0x3f803f80, v160\n" \
  "v_dot2c_f32_bf16 v30, v160, v160\n" \
  "v_dot2c_f32_bf16 v31, 0x3f803f80, v48\n" \
  "v_dot2c_f32_bf16 v27, 0x3f803f80, v164\n" \
  "v_dot2c_f32_bf16 v28, v164, v164\n" \
  "v_dot2c_f32_bf16 v29, 0x3f803f80, v161\n" \
  "v_dot2c_f32_bf16 v30, v161, v161\n" \
  "v_dot2c_f32_bf16 v31, 0x3f803f80, v49\n" \
  "v_dot2c_f32_bf16 v27, 0x3f803f80, v165\n" \
  "v_dot2c_f32_bf16 v28, v165, v165\n" \
  "v_dot2c_f32_bf16 v29, 0x3f803f80, v162\n" \
  "v_dot2c_f32_bf16 v30, v162, v162\n" \
  "v_dot2c_f32_bf16 v31, 0x3f803f80, v50\n" \
  "v_dot2c_f32_bf16 v27, 0x3f803f80, v166\n" \
  "v_dot2c_f32_bf16 v28, v166, v166\n" \
  "v_dot2c_f32_bf16 v29, 0x3f803f80, v163\n" \
  "v_dot2c_f32_bf16 v30, v163, v163\n" \
  "v_dot2c_f32_bf16 v31, 0x3f803f80, v51\n" \
  "v_dot2c_f32_bf16 v27, 0x3f803f80, v167\n" \
  "v_dot2c_f32_bf16 v28, v167, v167\n" \
  "s_waitcnt lgkmcnt(0)\n" \
  "v_mfma_f32_32x32x16_bf16 v[32:47], v[48:51], v[160:163], v[32:47]\n" \
  "v_mfma_f32_32x32x16_bf16 v[32:47], v[52:55], v[164:167], v[32:47]\n" \
  "v_mfma_f32_32x32x16_bf16 v[32:47], v[56:59], v[168:171], v[32:47]\n" \
  "v_mfma_f32_32x32x16_bf16 v[32:47], v[60:63], v[172:175], v[32:47]\n" \
  "s_cmp_lt_u32 s36, 4\n" \
  "s_cbranch_scc1 fbk_sk4b\n" \
  "s_waitcnt vmcnt(6)\n" \
  "s_barrier\n" \
  "fbk_sk4b:\n" \
  "s_mov_b32 m0, s87\n" \
  "s_nop 0\n" \
  "buffer_load_dwordx4 v0, s[8:11], s83 offen lds\n" \
  "ds_read_b128 v[176:179], v4 offset:16384\n" \
  "ds_read_b64_tr_b16 v[64:65], v8 offset:8192\n" \
  "ds_read_b64_tr_b16 v[66:67], v8 offset:8704\n" \
  "ds_read_b128 v[180:183], v5 offset:16384\n" \
  "ds_read_b64_tr_b16 v[68:69], v9 offset:8192\n" \
  "ds_read_b64_tr_b16 v[70:71], v9 offset:8704\n" \
  "ds_read_b128 v[184:187], v6 offset:16384\n" \
  "ds_read_b64_tr_b16 v[72:73], v10 offset:8192\n" \
  "ds_read_b64_tr_b16 v[74:75], v10 offset:8704\n" \
  "ds_read_b128 v[188:191], v7 offset:16384\n" \
  "ds_read_b64_tr_b16 v[76:77], v11 offset:8192\n" \
  "ds_read_b64_tr_b16 v[78:79], v11 offset:8704\n" \
    \
  "s_cmp_ge_u32 s36, 4\n" \
  "s_cbranch_scc1 fbk_sk5a\n" \
  "s_waitcnt vmcnt(4) lgkmcnt(0)\n" \
  "s_barrier\n" \
  "fbk_sk5a:\n" \
  "s_waitcnt lgkmcnt(8)\n" \
  "v_dot2c_f32_bf16 v29, 0x3f803f80, v176\n" \
  "v_dot2c_f32_bf16 v30, v176, v176\n" \
  "v_dot2c_f32_bf16 v31, 0x3f803f80, v64\n" \
  "v_dot2c_f32_bf16 v27, 0x3f803f80, v180\n" \
  "v_dot2c_f32_bf16 v28, v180, v180\n" \
  "v_dot2c_f32_bf16 v29, 0x3f803f80, v177\n" \
  "v_dot2c_f32_bf16 v30, v177, v177\n" \
  "v_dot2c_f32_bf16 v31, 0x3f803f80, v65\n" \
  "v_dot2c_f32_bf16 v27, 0x3f803f80, v181\n" \
  "v_dot2c_f32_bf16 v28, v181, v181\n" \
  "v_dot2c_f32_bf16 v29, 0x3f803f80, v178\n" \
  "v_dot2c_f32_bf16 v30, v178, v178\n" \
  "v_dot2c_f32_bf16 v31, 0x3f803f80, v66\n" \
  "v_dot2c_f32_bf16 v27, 0x3f803f80, v182\n" \
  "v_dot2c_f32_bf16 v28, v182, v182\n" \
  "v_dot2c_f32_bf16 v29, 0x3f803f80, v179\n" \
  "v_dot2c_f32_bf16 v30, v179, v179\n" \
  "v_dot2c_f32_bf16 v31, 0x3f803f80, v67\n" \
  "v_dot2c_f32_bf16 v27, 0x3f803f80, v183\n" \
  "v_dot2c_f32_bf16 v28, v183, v183\n" \
  "s_waitcnt lgkmcnt(0)\n" \
  "v_mfma_f32_32x32x16_bf16 v[32:47], v[64:67], v[176:179], v[32:47]\n" \
  "v_mfma_f32_32x32x16_bf16 v[32:47], v[68:71], v[180:183], v[32:47]\n" \
  "v_mfma_f32_32x32x16_bf16 v[32:47], v[72:75], v[184:187], v[32:47]\n" \
  "v_mfma_f32_32x32x16_bf16 v[32:47], v[76:79], v[188:191], v[32:47]\n" \
  "s_cmp_lt_u32 s36, 4\n" \
  "s_cbranch_scc1 fbk_sk5b\n" \
  "s_waitcnt vmcnt(4)\n" \
  "s_barrier\n" \
  "fbk_sk5b:\n" \
  "s_mov_b32 m0, s88\n" \
  "s_nop 0\n" \
  "buffer_load_dwordx4 v0, s[8:11], s84 offen lds\n" \
  "ds_read_b128 v[192:195], v4 offset:32768\n" \
  "ds_read_b64_tr_b16 v[48:49], v8 offset:16384\n" \
  "ds_read_b64_tr_b16 v[50:51], v8 offset:16896\n" \
  "ds_read_b128 v[196:199], v5 offset:32768\n" \
  "ds_read_b64_tr_b16 v[52:53], v9 offset:16384\n" \
  "ds_read_b64_tr_b16 v[54:55], v9 offset:16896\n" \
  "ds_read_b128 v[200:203], v6 offset:32768\n" \
  "ds_read_b64_tr_b16 v[56:57], v10 offset:16384\n" \
  "ds_read_b64_tr_b16 v[58:59], v10 offset:16896\n" \
  "ds_read_b128 v[204:207], v7 offset:32768\n" \
  "ds_read_b64_tr_b16 v[60:61], v11 offset:16384\n" \
  "ds_read_b64_tr_b16 v[62:63], v11 offset:16896\n" \
    \
  "s_cmp_ge_u32 s36, 4\n" \
  "s_cbranch_scc1 fbk_sk6a\n" \
  "s_waitcnt vmcnt(2) lgkmcnt(0)\n" \
  "s_barrier\n" \
  "fbk_sk6a:\n" \
  "s_waitcnt lgkmcnt(8)\n" \
  "v_dot2c_f32_bf16 v29, 0x3f803f80, v192\n" \
  "v_dot2c_f32_bf16 v30, v192, v192\n" \
  "v_dot2c_f32_bf16 v31, 0x3f803f80, v48\n" \
  "v_dot2c_f32_bf16 v27, 0x3f803f80, v196\n" \
  "v_dot2c_f32_bf16 v28, v196, v196\n" \
  "v_dot2c_f32_bf16 v29, 0x3f803f80, v193\n" \
  "v_dot2c_f32_bf16 v30, v193, v193\n" \
  "v_dot2c_f32_bf16 v31, 0x3f803f80, v49\n" \
  "v_dot2c_f32_bf16 v27, 0x3f803f80, v197\n" \
  "v_dot2c_f32_bf16 v28, v197, v197\n" \
  "v_dot2c_f32_bf16 v29, 0x3f803f80, v194\n" \
  "v_dot2c_f32_bf16 v30, v194, v194\n" \
  "v_dot2c_f32_bf16 v31, 0x3f803f80, v50\n" \
  "v_dot2c_f32_bf16 v27, 0x3f803f80, v198\n" \
  "v_dot2c_f32_bf16 v28, v198, v198\n" \
  "v_dot2c_f32_bf16 v29, 0x3f803f80, v195\n" \
  "v_dot2c_f32_bf16 v30, v195, v195\n" \
  "v_dot2c_f32_bf16 v31, 0x3f803f80, v51\n" \
  "v_dot2c_f32_bf16 v27, 0x3f803f80, v199\n" \
  "v_dot2c_f32_bf16 v28, v199, v199\n" \
  "s_waitcnt lgkmcnt(0)\n" \
  "v_mfma_f32_32x32x16_bf16 v[32:47], v[48:51], v[192:195], v[32:47]\n" \
  "v_mfma_f32_32x32x16_bf16 v[32:47], v[52:55], v[196:199], v[32:47]\n" \
  "v_mfma_f32_32x32x16_bf16 v[32:47], v[56:59], v[200:203], v[32:47]\n" \
  "v_mfma_f32_32x32x16_bf16 v[32:47], v[60:63], v[204:207], v[32:47]\n" \
  "s_cmp_lt_u32 s36, 4\n" \
  "s_cbranch_scc1 fbk_sk6b\n" \
  "s_waitcnt vmcnt(2)\n" \
  "s_barrier\n" \
  "fbk_sk6b:\n" \
  "s_mov_b32 m0, s89\n" \
  "s_nop 0\n" \
  "buffer_load_dwordx4 v0, s[8:11], s85 offen lds\n" \
  "ds_read_b128 v[208:211], v4 offset:49152\n" \
  "ds_read_b64_tr_b16 v[64:65], v8 offset:24576\n" \
  "ds_read_b64_tr_b16 v[66:67], v8 offset:25088\n" \
  "ds_read_b128 v[212:215], v5 offset:49152\n" \
  "ds_read_b64_tr_b16 v[68:69], v9 offset:24576\n" \
  "ds_read_b64_tr_b16 v[70:71], v9 offset:25088\n" \
  "s_mov_b32 m0, s90\n" \
  "s_nop 0\n" \
  "buffer_load_dwordx4 v0, s[8:11], s86 offen lds\n" \
  "ds_read_b128 v[216:219], v6 offset:49152\n" \
  "ds_read_b64_tr_b16 v[72:73], v10 offset:24576\n" \
  "ds_read_b64_tr_b16 v[74:75], v10 offset:25088\n" \
  "ds_read_b128 v[220:223], v7 offset:49152\n" \
  "ds_read_b64_tr_b16 v[76:77], v11 offset:24576\n" \
  "ds_read_b64_tr_b16 v[78:79], v11 offset:25088\n" \
    \
  "s_waitcnt lgkmcnt(11)\n" \
  "v_dot2c_f32_bf16 v29, 0x3f803f80, v208\n" \
  "v_dot2c_f32_bf16 v30, v208, v208\n" \
  "s_waitcnt lgkmcnt(10)\n" \
  "v_dot2c_f32_bf16 v31, 0x3f803f80, v64\n" \
  "s_waitcnt lgkmcnt(8)\n" \
  "v_dot2c_f32_bf16 v27, 0x3f803f80, v212\n" \
  "v_dot2c_f32_bf16 v28, v212, v212\n" \
  "v_dot2c_f32_bf16 v29, 0x3f803f80, v209\n" \
  "v_dot2c_f32_bf16 v30, v209, v209\n" \
  "v_dot2c_f32_bf16 v31, 0x3f803f80, v65\n" \
  "v_dot2c_f32_bf16 v27, 0x3f803f80, v213\n" \
  "v_dot2c_f32_bf16 v28, v213, v213\n" \
  "v_dot2c_f32_bf16 v29, 0x3f803f80, v210\n" \
  "v_dot2c_f32_bf16 v30, v210, v210\n" \
  "v_dot2c_f32_bf16 v31, 0x3f803f80, v66\n" \
  "v_dot2c_f32_bf16 v27, 0x3f803f80, v214\n" \
  "v_dot2c_f32_bf16 v28, v214, v214\n" \
  "v_dot2c_f32_bf16 v29, 0x3f803f80, v211\n" \
  "v_dot2c_f32_bf16 v30, v211, v211\n" \
  "v_dot2c_f32_bf16 v31, 0x3f803f80, v67\n" \
  "v_dot2c_f32_bf16 v27, 0x3f803f80, v215\n" \
  "v_dot2c_f32_bf16 v28, v215, v215\n" \
  "s_nop 1\n" \
  "v_add_f32 v29, v29, v27\n" \
  "v_add_f32 v30, v30, v28\n" \
  "ds_write2_b32 v16, v29, v30 offset1:1\n" \
  "ds_write_b32 v18, v31\n" \
  "v_mfma_f32_32x32x16_bf16 v[32:47], v[64:67], v[208:211], v[32:47]\n" \
  "s_waitcnt lgkmcnt(8)\n" \
  "v_mfma_f32_32x32x16_bf16 v[32:47], v[68:71], v[212:215], v[32:47]\n" \
  "s_waitcnt lgkmcnt(0)\n" \
  "s_barrier\n" \
  "ds_read_b64 v[80:81], v17\n" \
  "ds_read_b128 v[84:87], v19\n" \
  "v_mfma_f32_32x32x16_bf16 v[32:47], v[72:75], v[216:219], v[32:47]\n" \
  "v_mfma_f32_32x32x16_bf16 v[32:47], v[76:79], v[220:223], v[32:47]\n" \
  "s_waitcnt lgkmcnt(1)\n" \
  "v_add_f32 v29, v29, v80\n" \
  "v_add_f32 v30, v30, v81\n" \
  "v_mov_b32 v80, v29\n" \
  "v_mov_b32 v81, v30\n" \
  "s_waitcnt lgkmcnt(0)\n" \
  "v_add_f32 v84, v84, v85\n" \
  "v_add_f32 v86, v86, v87\n" \
  "v_permlane32_swap_b32 v29, v80\n" \
  "v_permlane32_swap_b32 v30, v81\n" \
  "v_add_f32 v29, v29, v80\n" \
  "v_add_f32 v30, v30, v81\n" \
  "v_mul_f32 v29, 0xbb000000, v29\n" \
  "v_add_f32 v31, v84, v86\n" \
  "v_cvt_pk_bf16_f32 v85, v29, v29\n" \
  "v_cvt_pk_bf16_f32 v48, v31, v31\n" \
  "v_and_b32 v85, 0xffff0000, v85\n" \
  "v_and_b32 v84, 0xffff0000, v48\n" \
  "v_sub_f32 v85, v29, v85\n" \
  "v_sub_f32 v84, v31, v84\n" \
  "v_cvt_pk_bf16_f32 v16, v29, v85\n" \
  "v_cvt_pk_bf16_f32 v49, v84, v84\n" \
  "v_cvt_pk_bf16_f32 v17, v29, v85\n" \
    \
  "v_mul_f32 v30, 0x3b000000, v30\n" \
  "v_fma_f32 v30, -v29, v29, v30\n" \
  "v_mfma_f32_32x32x8_bf16 v[32:47], v[48:49], v[16:17], v[32:47]\n" \
  "v_max_f32 v30, 0, v30\n" \
  "v_add_f32 v30, 0x3727c5ac, v30\n" \
  "v_rsq_f32 v30, v30\n" \
  "s_nop 8\n" \
  "v_pk_mul_f32 v[32:33], v[32:33], v[30:31] op_sel_hi:[1,0]\n" \
  "v_pk_mul_f32 v[34:35], v[34:35], v[30:31] op_sel_hi:[1,0]\n" \
  "v_pk_mul_f32 v[36:37], v[36:37], v[30:31] op_sel_hi:[1,0]\n" \
  "v_pk_mul_f32 v[38:39], v[38:39], v[30:31] op_sel_hi:[1,0]\n" \
  "v_pk_mul_f32 v[40:41], v[40:41], v[30:31] op_sel_hi:[1,0]\n" \
  "v_pk_mul_f32 v[42:43], v[42:43], v[30:31] op_sel_hi:[1,0]\n" \
  "v_pk_mul_f32 v[44:45], v[44:45], v[30:31] op_sel_hi:[1,0]\n" \
  "v_pk_mul_f32 v[46:47], v[46:47], v[30:31] op_sel_hi:[1,0]\n" \
  "s_branch fbk_a3\n" \
  "fbk_av:\n" \
    \
  "v_and_b32 v20, 16, v250\n" \
  "v_lshl_or_b32 v20, v20, 13, v233\n" \
  "v_add_u32 v21, 512, v20\n" \
    \
  "s_cmp_ge_u32 s36, 4\n" \
  "s_cbranch_scc1 fbk_sv0a\n" \
  "s_waitcnt vmcnt(6) lgkmcnt(0)\n" \
  "s_barrier\n" \
  "fbk_sv0a:\n" \
  "s_waitcnt lgkmcnt(8)\n" \
  "v_dot2c_f32_bf16 v29, 0x3f803f80, v96\n" \
  "v_dot2c_f32_bf16 v30, v96, v96\n" \
  "v_dot2c_f32_bf16 v31, 0x3f803f80, v48\n" \
  "v_dot2c_f32_bf16 v27, 0x3f803f80, v100\n" \
  "v_dot2c_f32_bf16 v28, v100, v100\n" \
  "v_dot2c_f32_bf16 v29, 0x3f803f80, v97\n" \
  "v_dot2c_f32_bf16 v30, v97, v97\n" \
  "v_dot2c_f32_bf16 v31, 0x3f803f80, v49\n" \
  "v_dot2c_f32_bf16 v27, 0x3f803f80, v101\n" \
  "v_dot2c_f32_bf16 v28, v101, v101\n" \
  "v_dot2c_f32_bf16 v29, 0x3f803f80, v98\n" \
  "v_dot2c_f32_bf16 v30, v98, v98\n" \
  "v_dot2c_f32_bf16 v31, 0x3f803f80, v50\n" \
  "v_dot2c_f32_bf16 v27, 0x3f803f80, v102\n" \
  "v_dot2c_f32_bf16 v28, v102, v102\n" \
  "v_dot2c_f32_bf16 v29, 0x3f803f80, v99\n" \
  "v_dot2c_f32_bf16 v30, v99, v99\n" \
  "v_dot2c_f32_bf16 v31, 0x3f803f80, v51\n" \
  "v_dot2c_f32_bf16 v27, 0x3f803f80, v103\n" \
  "v_dot2c_f32_bf16 v28, v103, v103\n" \
  "s_waitcnt lgkmcnt(0)\n" \
  "v_mfma_f32_32x32x16_bf16 v[32:47], v[96:99], v[48:51], 0\n" \
  "v_mfma_f32_32x32x16_bf16 v[32:47], v[100:103], v[52:55], v[32:47]\n" \
  "v_mfma_f32_32x32x16_bf16 v[32:47], v[104:107], v[56:59], v[32:47]\n" \
  "v_mfma_f32_32x32x16_bf16 v[32:47], v[108:111], v[60:63], v[32:47]\n" \
  "s_cmp_lt_u32 s36, 4\n" \
  "s_cbranch_scc1 fbk_sv0b\n" \
  "s_waitcnt vmcnt(6)\n" \
  "s_barrier\n" \
  "fbk_sv0b:\n" \
  "s_add_u32 m0, s53, 0\n" \
  "s_xor_b32 s50, s51, 512\n" \
  "buffer_load_dwordx4 v1, s[4:7], s50 offen lds\n" \
  "ds_read_b128 v[112:115], v4 offset:16384\n" \
  "ds_read_b64_tr_b16 v[64:65], v8 offset:8192\n" \
  "ds_read_b64_tr_b16 v[66:67], v8 offset:8704\n" \
  "ds_read_b128 v[116:119], v5 offset:16384\n" \
  "ds_read_b64_tr_b16 v[68:69], v9 offset:8192\n" \
  "ds_read_b64_tr_b16 v[70:71], v9 offset:8704\n" \
  "buffer_load_dwordx4 v2, s[4:7], s50 offen offset:1024 lds\n" \
  "ds_read_b128 v[120:123], v6 offset:16384\n" \
  "ds_read_b64_tr_b16 v[72:73], v10 offset:8192\n" \
  "ds_read_b64_tr_b16 v[74:75], v10 offset:8704\n" \
  "ds_read_b128 v[124:127], v7 offset:16384\n" \
  "ds_read_b64_tr_b16 v[76:77], v11 offset:8192\n" \
  "ds_read_b64_tr_b16 v[78:79], v11 offset:8704\n" \
  "s_add_u32 m0, s54, 0\n" \
  "s_mul_i32 s50, s50, 1536\n" \
  "buffer_load_dwordx4 v3, s[8:11], s50 offen lds\n" \
    \
  "s_cmp_ge_u32 s36, 4\n" \
  "s_cbranch_scc1 fbk_sv1a\n" \
  "s_waitcnt vmcnt(6) lgkmcnt(0)\n" \
  "s_barrier\n" \
  "fbk_sv1a:\n" \
  "s_waitcnt lgkmcnt(8)\n" \
  "v_dot2c_f32_bf16 v29, 0x3f803f80, v112\n" \
  "v_dot2c_f32_bf16 v30, v112, v112\n" \
  "v_dot2c_f32_bf16 v31, 0x3f803f80, v64\n" \
  "v_dot2c_f32_bf16 v27, 0x3f803f80, v116\n" \
  "v_dot2c_f32_bf16 v28, v116, v116\n" \
  "v_dot2c_f32_bf16 v29, 0x3f803f80, v113\n" \
  "v_dot2c_f32_bf16 v30, v113, v113\n" \
  "v_dot2c_f32_bf16 v31, 0x3f803f80, v65\n" \
  "v_dot2c_f32_bf16 v27, 0x3f803f80, v117\n" \
  "v_dot2c_f32_bf16 v28, v117, v117\n" \
  "v_dot2c_f32_bf16 v29, 0x3f803f80, v114\n" \
  "v_dot2c_f32_bf16 v30, v114, v114\n" \
  "v_dot2c_f32_bf16 v31, 0x3f803f80, v66\n" \
  "v_dot2c_f32_bf16 v27, 0x3f803f80, v118\n" \
  "v_dot2c_f32_bf16 v28, v118, v118\n" \
  "v_dot2c_f32_bf16 v29, 0x3f803f80, v115\n" \
  "v_dot2c_f32_bf16 v30, v115, v115\n" \
  "v_dot2c_f32_bf16 v31, 0x3f803f80, v67\n" \
  "v_dot2c_f32_bf16 v27, 0x3f803f80, v119\n" \
  "v_dot2c_f32_bf16 v28, v119, v119\n" \
  "s_waitcnt lgkmcnt(0)\n" \
  "v_mfma_f32_32x32x16_bf16 v[32:47], v[112:115], v[64:67], v[32:47]\n" \
  "v_mfma_f32_32x32x16_bf16 v[32:47], v[116:119], v[68:71], v[32:47]\n" \
  "v_mfma_f32_32x32x16_bf16 v[32:47], v[120:123], v[72:75], v[32:47]\n" \
  "v_mfma_f32_32x32x16_bf16 v[32:47], v[124:127], v[76:79], v[32:47]\n" \
  "s_cmp_lt_u32 s36, 4\n" \
  "s_cbranch_scc1 fbk_sv1b\n" \
  "s_waitcnt vmcnt(6)\n" \
  "s_barrier\n" \
  "fbk_sv1b:\n" \
  "s_add_u32 m0, s53, 16384\n" \
  "s_xor_b32 s50, s51, 640\n" \
  "buffer_load_dwordx4 v1, s[4:7], s50 offen lds\n" \
  "ds_read_b128 v[128:131], v4 offset:32768\n" \
  "ds_read_b64_tr_b16 v[48:49], v8 offset:16384\n" \
  "ds_read_b64_tr_b16 v[50:51], v8 offset:16896\n" \
  "ds_read_b128 v[132:135], v5 offset:32768\n" \
  "ds_read_b64_tr_b16 v[52:53], v9 offset:16384\n" \
  "ds_read_b64_tr_b16 v[54:55], v9 offset:16896\n" \
  "buffer_load_dwordx4 v2, s[4:7], s50 offen offset:1024 lds\n" \
  "ds_read_b128 v[136:139], v6 offset:32768\n" \
  "ds_read_b64_tr_b16 v[56:57], v10 offset:16384\n" \
  "ds_read_b64_tr_b16 v[58:59], v10 offset:16896\n" \
  "ds_read_b128 v[140:143], v7 offset:32768\n" \
  "ds_read_b64_tr_b16 v[60:61], v11 offset:16384\n" \
  "ds_read_b64_tr_b16 v[62:63], v11 offset:16896\n" \
  "s_add_u32 m0, s54, 8192\n" \
  "s_mul_i32 s50, s50, 1536\n" \
  "buffer_load_dwordx4 v3, s[8:11], s50 offen lds\n" \
    \
  "s_cmp_ge_u32 s36, 4\n" \
  "s_cbranch_scc1 fbk_sv2a\n" \
  "s_waitcnt vmcnt(6) lgkmcnt(0)\n" \
  "s_barrier\n" \
  "fbk_sv2a:\n" \
  "s_waitcnt lgkmcnt(8)\n" \
  "v_dot2c_f32_bf16 v29, 0x3f803f80, v128\n" \
  "v_dot2c_f32_bf16 v30, v128, v128\n" \
  "v_dot2c_f32_bf16 v31, 0x3f803f80, v48\n" \
  "v_dot2c_f32_bf16 v27, 0x3f803f80, v132\n" \
  "v_dot2c_f32_bf16 v28, v132, v132\n" \
  "v_dot2c_f32_bf16 v29, 0x3f803f80, v129\n" \
  "v_dot2c_f32_bf16 v30, v129, v129\n" \
  "v_dot2c_f32_bf16 v31, 0x3f803f80, v49\n" \
  "v_dot2c_f32_bf16 v27, 0x3f803f80, v133\n" \
  "v_dot2c_f32_bf16 v28, v133, v133\n" \
  "v_dot2c_f32_bf16 v29, 0x3f803f80, v130\n" \
  "v_dot2c_f32_bf16 v30, v130, v130\n" \
  "v_dot2c_f32_bf16 v31, 0x3f803f80, v50\n" \
  "v_dot2c_f32_bf16 v27, 0x3f803f80, v134\n" \
  "v_dot2c_f32_bf16 v28, v134, v134\n" \
  "v_dot2c_f32_bf16 v29, 0x3f803f80, v131\n" \
  "v_dot2c_f32_bf16 v30, v131, v131\n" \
  "v_dot2c_f32_bf16 v31, 0x3f803f80, v51\n" \
  "v_dot2c_f32_bf16 v27, 0x3f803f80, v135\n" \
  "v_dot2c_f32_bf16 v28, v135, v135\n" \
  "s_waitcnt lgkmcnt(0)\n" \
  "v_mfma_f32_32x32x16_bf16 v[32:47], v[128:131], v[48:51], v[32:47]\n" \
  "v_mfma_f32_32x32x16_bf16 v[32:47], v[132:135], v[52:55], v[32:47]\n" \
  "v_mfma_f32_32x32x16_bf16 v[32:47], v[136:139], v[56:59], v[32:47]\n" \
  "v_mfma_f32_32x32x16_bf16 v[32:47], v[140:143], v[60:63], v[32:47]\n" \
  "s_cmp_lt_u32 s36, 4\n" \
  "s_cbranch_scc1 fbk_sv2b\n" \
  "s_waitcnt vmcnt(6)\n" \
  "s_barrier\n" \
  "fbk_sv2b:\n" \
  "s_add_u32 m0, s53, 32768\n" \
  "s_xor_b32 s50, s51, 768\n" \
  "buffer_load_dwordx4 v1, s[4:7], s50 offen lds\n" \
  "ds_read_b128 v[144:147], v4 offset:49152\n" \
  "ds_read_b64_tr_b16 v[64:65], v8 offset:24576\n" \
  "ds_read_b64_tr_b16 v[66:67], v8 offset:25088\n" \
  "ds_read_b128 v[148:151], v5 offset:49152\n" \
  "ds_read_b64_tr_b16 v[68:69], v9 offset:24576\n" \
  "ds_read_b64_tr_b16 v[70:71], v9 offset:25088\n" \
  "buffer_load_dwordx4 v2, s[4:7], s50 offen offset:1024 lds\n" \
  "ds_read_b128 v[152:155], v6 offset:49152\n" \
  "ds_read_b64_tr_b16 v[72:73], v10 offset:24576\n" \
  "ds_read_b64_tr_b16 v[74:75], v10 offset:25088\n" \
  "ds_read_b128 v[156:159], v7 offset:49152\n" \
  "ds_read_b64_tr_b16 v[76:77], v11 offset:24576\n" \
  "ds_read_b64_tr_b16 v[78:79], v11 offset:25088\n" \
  "s_add_u32 m0, s54, 16384\n" \
  "s_mul_i32 s50, s50, 1536\n" \
  "buffer_load_dwordx4 v3, s[8:11], s50 offen lds\n" \
    \
  "s_cmp_ge_u32 s36, 4\n" \
  "s_cbranch_scc1 fbk_sv3a\n" \
  "s_waitcnt vmcnt(6) lgkmcnt(0)\n" \
  "s_barrier\n" \
  "fbk_sv3a:\n" \
  "s_waitcnt lgkmcnt(8)\n" \
  "v_dot2c_f32_bf16 v29, 0x3f803f80, v144\n" \
  "v_dot2c_f32_bf16 v30, v144, v144\n" \
  "v_dot2c_f32_bf16 v31, 0x3f803f80, v64\n" \
  "v_dot2c_f32_bf16 v27, 0x3f803f80, v148\n" \
  "v_dot2c_f32_bf16 v28, v148, v148\n" \
  "v_dot2c_f32_bf16 v29, 0x3f803f80, v145\n" \
  "v_dot2c_f32_bf16 v30, v145, v145\n" \
  "v_dot2c_f32_bf16 v31, 0x3f803f80, v65\n" \
  "v_dot2c_f32_bf16 v27, 0x3f803f80, v149\n" \
  "v_dot2c_f32_bf16 v28, v149, v149\n" \
  "v_dot2c_f32_bf16 v29, 0x3f803f80, v146\n" \
  "v_dot2c_f32_bf16 v30, v146, v146\n" \
  "v_dot2c_f32_bf16 v31, 0x3f803f80, v66\n" \
  "v_dot2c_f32_bf16 v27, 0x3f803f80, v150\n" \
  "v_dot2c_f32_bf16 v28, v150, v150\n" \
  "v_dot2c_f32_bf16 v29, 0x3f803f80, v147\n" \
  "v_dot2c_f32_bf16 v30, v147, v147\n" \
  "v_dot2c_f32_bf16 v31, 0x3f803f80, v67\n" \
  "v_dot2c_f32_bf16 v27, 0x3f803f80, v151\n" \
  "v_dot2c_f32_bf16 v28, v151, v151\n" \
  "s_waitcnt lgkmcnt(0)\n" \
  "v_mfma_f32_32x32x16_bf16 v[32:47], v[144:147], v[64:67], v[32:47]\n" \
  "v_mfma_f32_32x32x16_bf16 v[32:47], v[148:151], v[68:71], v[32:47]\n" \
  "v_mfma_f32_32x32x16_bf16 v[32:47], v[152:155], v[72:75], v[32:47]\n" \
  "v_mfma_f32_32x32x16_bf16 v[32:47], v[156:159], v[76:79], v[32:47]\n" \
  "s_cmp_lt_u32 s36, 4\n" \
  "s_cbranch_scc1 fbk_sv3b\n" \
  "s_waitcnt vmcnt(6)\n" \
  "s_barrier\n" \
  "fbk_sv3b:\n" \
  "s_add_u32 m0, s53, 49152\n" \
  "s_xor_b32 s50, s51, 896\n" \
  "buffer_load_dwordx4 v1, s[4:7], s50 offen lds\n" \
  "ds_read_b128 v[160:163], v4 offset:0\n" \
  "ds_read_b64_tr_b16 v[48:49], v8 offset:0\n" \
  "ds_read_b64_tr_b16 v[50:51], v8 offset:512\n" \
  "ds_read_b128 v[164:167], v5 offset:0\n" \
  "ds_read_b64_tr_b16 v[52:53], v9 offset:0\n" \
  "ds_read_b64_tr_b16 v[54:55], v9 offset:512\n" \
  "buffer_load_dwordx4 v2, s[4:7], s50 offen offset:1024 lds\n" \
  "ds_read_b128 v[168:171], v6 offset:0\n" \
  "ds_read_b64_tr_b16 v[56:57], v10 offset:0\n" \
  "ds_read_b64_tr_b16 v[58:59], v10 offset:512\n" \
  "ds_read_b128 v[172:175], v7 offset:0\n" \
  "ds_read_b64_tr_b16 v[60:61], v11 offset:0\n" \
  "ds_read_b64_tr_b16 v[62:63], v11 offset:512\n" \
  "s_add_u32 m0, s54, 24576\n" \
  "s_mul_i32 s50, s50, 1536\n" \
  "buffer_load_dwordx4 v3, s[8:11], s50 offen lds\n" \
    \
  "s_cmp_ge_u32 s36, 4\n" \
  "s_cbranch_scc1 fbk_sv4a\n" \
  "s_waitcnt vmcnt(6) lgkmcnt(0)\n" \
  "s_barrier\n" \
  "fbk_sv4a:\n" \
  "s_waitcnt lgkmcnt(8)\n" \
  "v_dot2c_f32_bf16 v29, 0x3f803f80, v160\n" \
  "v_dot2c_f32_bf16 v30, v160, v160\n" \
  "v_dot2c_f32_bf16 v31, 0x3f803f80, v48\n" \
  "v_dot2c_f32_bf16 v27, 0x3f803f80, v164\n" \
  "v_dot2c_f32_bf16 v28, v164, v164\n" \
  "v_dot2c_f32_bf16 v29, 0x3f803f80, v161\n" \
  "v_dot2c_f32_bf16 v30, v161, v161\n" \
  "v_dot2c_f32_bf16 v31, 0x3f803f80, v49\n" \
  "v_dot2c_f32_bf16 v27, 0x3f803f80, v165\n" \
  "v_dot2c_f32_bf16 v28, v165, v165\n" \
  "v_dot2c_f32_bf16 v29, 0x3f803f80, v162\n" \
  "v_dot2c_f32_bf16 v30, v162, v162\n" \
  "v_dot2c_f32_bf16 v31, 0x3f803f80, v50\n" \
  "v_dot2c_f32_bf16 v27, 0x3f803f80, v166\n" \
  "v_dot2c_f32_bf16 v28, v166, v166\n" \
  "v_dot2c_f32_bf16 v29, 0x3f803f80, v163\n" \
  "v_dot2c_f32_bf16 v30, v163, v163\n" \
  "v_dot2c_f32_bf16 v31, 0x3f803f80, v51\n" \
  "v_dot2c_f32_bf16 v27, 0x3f803f80, v167\n" \
  "v_dot2c_f32_bf16 v28, v167, v167\n" \
  "s_waitcnt lgkmcnt(0)\n" \
  "v_mfma_f32_32x32x16_bf16 v[32:47], v[160:163], v[48:51], v[32:47]\n" \
  "v_mfma_f32_32x32x16_bf16 v[32:47], v[164:167], v[52:55], v[32:47]\n" \
  "v_mfma_f32_32x32x16_bf16 v[32:47], v[168:171], v[56:59], v[32:47]\n" \
  "v_mfma_f32_32x32x16_bf16 v[32:47], v[172:175], v[60:63], v[32:47]\n" \
  "s_cmp_lt_u32 s36, 4\n" \
  "s_cbranch_scc1 fbk_sv4b\n" \
  "s_waitcnt vmcnt(6)\n" \
  "s_barrier\n" \
  "fbk_sv4b:\n" \
  "s_mov_b32 m0, s87\n" \
  "s_nop 0\n" \
  "buffer_load_dwordx4 v0, s[8:11], s83 offen lds\n" \
  "ds_read_b128 v[176:179], v4 offset:16384\n" \
  "ds_read_b64_tr_b16 v[64:65], v8 offset:8192\n" \
  "ds_read_b64_tr_b16 v[66:67], v8 offset:8704\n" \
  "ds_read_b128 v[180:183], v5 offset:16384\n" \
  "ds_read_b64_tr_b16 v[68:69], v9 offset:8192\n" \
  "ds_read_b64_tr_b16 v[70:71], v9 offset:8704\n" \
  "ds_read_b128 v[184:187], v6 offset:16384\n" \
  "ds_read_b64_tr_b16 v[72:73], v10 offset:8192\n" \
  "ds_read_b64_tr_b16 v[74:75], v10 offset:8704\n" \
  "ds_read_b128 v[188:191], v7 offset:16384\n" \
  "ds_read_b64_tr_b16 v[76:77], v11 offset:8192\n" \
  "ds_read_b64_tr_b16 v[78:79], v11 offset:8704\n" \
    \
  "s_cmp_ge_u32 s36, 4\n" \
  "s_cbranch_scc1 fbk_sv5a\n" \
  "s_waitcnt vmcnt(4) lgkmcnt(0)\n" \
  "s_barrier\n" \
  "fbk_sv5a:\n" \
  "s_waitcnt lgkmcnt(8)\n" \
  "v_dot2c_f32_bf16 v29, 0x3f803f80, v176\n" \
  "v_dot2c_f32_bf16 v30, v176, v176\n" \
  "v_dot2c_f32_bf16 v31, 0x3f803f80, v64\n" \
  "v_dot2c_f32_bf16 v27, 0x3f803f80, v180\n" \
  "v_dot2c_f32_bf16 v28, v180, v180\n" \
  "v_dot2c_f32_bf16 v29, 0x3f803f80, v177\n" \
  "v_dot2c_f32_bf16 v30, v177, v177\n" \
  "v_dot2c_f32_bf16 v31, 0x3f803f80, v65\n" \
  "v_dot2c_f32_bf16 v27, 0x3f803f80, v181\n" \
  "v_dot2c_f32_bf16 v28, v181, v181\n" \
  "v_dot2c_f32_bf16 v29, 0x3f803f80, v178\n" \
  "v_dot2c_f32_bf16 v30, v178, v178\n" \
  "v_dot2c_f32_bf16 v31, 0x3f803f80, v66\n" \
  "v_dot2c_f32_bf16 v27, 0x3f803f80, v182\n" \
  "v_dot2c_f32_bf16 v28, v182, v182\n" \
  "v_dot2c_f32_bf16 v29, 0x3f803f80, v179\n" \
  "v_dot2c_f32_bf16 v30, v179, v179\n" \
  "v_dot2c_f32_bf16 v31, 0x3f803f80, v67\n" \
  "v_dot2c_f32_bf16 v27, 0x3f803f80, v183\n" \
  "v_dot2c_f32_bf16 v28, v183, v183\n" \
  "s_waitcnt lgkmcnt(0)\n" \
  "v_mfma_f32_32x32x16_bf16 v[32:47], v[176:179], v[64:67], v[32:47]\n" \
  "v_mfma_f32_32x32x16_bf16 v[32:47], v[180:183], v[68:71], v[32:47]\n" \
  "v_mfma_f32_32x32x16_bf16 v[32:47], v[184:187], v[72:75], v[32:47]\n" \
  "v_mfma_f32_32x32x16_bf16 v[32:47], v[188:191], v[76:79], v[32:47]\n" \
  "s_cmp_lt_u32 s36, 4\n" \
  "s_cbranch_scc1 fbk_sv5b\n" \
  "s_waitcnt vmcnt(4)\n" \
  "s_barrier\n" \
  "fbk_sv5b:\n" \
  "s_mov_b32 m0, s88\n" \
  "s_nop 0\n" \
  "buffer_load_dwordx4 v0, s[8:11], s84 offen lds\n" \
  "ds_read_b128 v[192:195], v4 offset:32768\n" \
  "ds_read_b64_tr_b16 v[48:49], v8 offset:16384\n" \
  "ds_read_b64_tr_b16 v[50:51], v8 offset:16896\n" \
  "ds_read_b128 v[196:199], v5 offset:32768\n" \
  "ds_read_b64_tr_b16 v[52:53], v9 offset:16384\n" \
  "ds_read_b64_tr_b16 v[54:55], v9 offset:16896\n" \
  "ds_read_b128 v[200:203], v6 offset:32768\n" \
  "ds_read_b64_tr_b16 v[56:57], v10 offset:16384\n" \
  "ds_read_b64_tr_b16 v[58:59], v10 offset:16896\n" \
  "ds_read_b128 v[204:207], v7 offset:32768\n" \
  "ds_read_b64_tr_b16 v[60:61], v11 offset:16384\n" \
  "ds_read_b64_tr_b16 v[62:63], v11 offset:16896\n" \
    \
  "s_cmp_ge_u32 s36, 4\n" \
  "s_cbranch_scc1 fbk_sv6a\n" \
  "s_waitcnt vmcnt(2) lgkmcnt(0)\n" \
  "s_barrier\n" \
  "fbk_sv6a:\n" \
  "s_waitcnt lgkmcnt(8)\n" \
  "v_dot2c_f32_bf16 v29, 0x3f803f80, v192\n" \
  "v_dot2c_f32_bf16 v30, v192, v192\n" \
  "v_dot2c_f32_bf16 v31, 0x3f803f80, v48\n" \
  "v_dot2c_f32_bf16 v27, 0x3f803f80, v196\n" \
  "v_dot2c_f32_bf16 v28, v196, v196\n" \
  "v_dot2c_f32_bf16 v29, 0x3f803f80, v193\n" \
  "v_dot2c_f32_bf16 v30, v193, v193\n" \
  "v_dot2c_f32_bf16 v31, 0x3f803f80, v49\n" \
  "v_dot2c_f32_bf16 v27, 0x3f803f80, v197\n" \
  "v_dot2c_f32_bf16 v28, v197, v197\n" \
  "v_dot2c_f32_bf16 v29, 0x3f803f80, v194\n" \
  "v_dot2c_f32_bf16 v30, v194, v194\n" \
  "v_dot2c_f32_bf16 v31, 0x3f803f80, v50\n" \
  "v_dot2c_f32_bf16 v27, 0x3f803f80, v198\n" \
  "v_dot2c_f32_bf16 v28, v198, v198\n" \
  "v_dot2c_f32_bf16 v29, 0x3f803f80, v195\n" \
  "v_dot2c_f32_bf16 v30, v195, v195\n" \
  "v_dot2c_f32_bf16 v31, 0x3f803f80, v51\n" \
  "v_dot2c_f32_bf16 v27, 0x3f803f80, v199\n" \
  "v_dot2c_f32_bf16 v28, v199, v199\n" \
  "s_waitcnt lgkmcnt(0)\n" \
  "v_mfma_f32_32x32x16_bf16 v[32:47], v[192:195], v[48:51], v[32:47]\n" \
  "v_mfma_f32_32x32x16_bf16 v[32:47], v[196:199], v[52:55], v[32:47]\n" \
  "v_mfma_f32_32x32x16_bf16 v[32:47], v[200:203], v[56:59], v[32:47]\n" \
  "v_mfma_f32_32x32x16_bf16 v[32:47], v[204:207], v[60:63], v[32:47]\n" \
  "s_cmp_lt_u32 s36, 4\n" \
  "s_cbranch_scc1 fbk_sv6b\n" \
  "s_waitcnt vmcnt(2)\n" \
  "s_barrier\n" \
  "fbk_sv6b:\n" \
  "s_mov_b32 m0, s89\n" \
  "s_nop 0\n" \
  "buffer_load_dwordx4 v0, s[8:11], s85 offen lds\n" \
  "ds_read_b128 v[208:211], v4 offset:49152\n" \
  "ds_read_b64_tr_b16 v[64:65], v8 offset:24576\n" \
  "ds_read_b64_tr_b16 v[66:67], v8 offset:25088\n" \
  "ds_read_b128 v[212:215], v5 offset:49152\n" \
  "ds_read_b64_tr_b16 v[68:69], v9 offset:24576\n" \
  "ds_read_b64_tr_b16 v[70:71], v9 offset:25088\n" \
  "s_mov_b32 m0, s90\n" \
  "s_nop 0\n" \
  "buffer_load_dwordx4 v0, s[8:11], s86 offen lds\n" \
  "ds_read_b128 v[216:219], v6 offset:49152\n" \
  "ds_read_b64_tr_b16 v[72:73], v10 offset:24576\n" \
  "ds_read_b64_tr_b16 v[74:75], v10 offset:25088\n" \
  "ds_read_b128 v[220:223], v7 offset:49152\n" \
  "ds_read_b64_tr_b16 v[76:77], v11 offset:24576\n" \
  "ds_read_b64_tr_b16 v[78:79], v11 offset:25088\n" \
    \
  "s_waitcnt lgkmcnt(11)\n" \
  "v_dot2c_f32_bf16 v29, 0x3f803f80, v208\n" \
  "v_dot2c_f32_bf16 v30, v208, v208\n" \
  "s_waitcnt lgkmcnt(10)\n" \
  "v_dot2c_f32_bf16 v31, 0x3f803f80, v64\n" \
  "s_waitcnt lgkmcnt(8)\n" \
  "v_dot2c_f32_bf16 v27, 0x3f803f80, v212\n" \
  "v_dot2c_f32_bf16 v28, v212, v212\n" \
  "v_dot2c_f32_bf16 v29, 0x3f803f80, v209\n" \
  "v_dot2c_f32_bf16 v30, v209, v209\n" \
  "v_dot2c_f32_bf16 v31, 0x3f803f80, v65\n" \
  "v_dot2c_f32_bf16 v27, 0x3f803f80, v213\n" \
  "v_dot2c_f32_bf16 v28, v213, v213\n" \
  "v_dot2c_f32_bf16 v29, 0x3f803f80, v210\n" \
  "v_dot2c_f32_bf16 v30, v210, v210\n" \
  "v_dot2c_f32_bf16 v31, 0x3f803f80, v66\n" \
  "v_dot2c_f32_bf16 v27, 0x3f803f80, v214\n" \
  "v_dot2c_f32_bf16 v28, v214, v214\n" \
  "v_dot2c_f32_bf16 v29, 0x3f803f80, v211\n" \
  "v_dot2c_f32_bf16 v30, v211, v211\n" \
  "v_dot2c_f32_bf16 v31, 0x3f803f80, v67\n" \
  "v_dot2c_f32_bf16 v27, 0x3f803f80, v215\n" \
  "v_dot2c_f32_bf16 v28, v215, v215\n" \
  "s_nop 1\n" \
  "v_add_f32 v29, v29, v27\n" \
  "v_add_f32 v30, v30, v28\n" \
  "ds_write2_b32 v16, v29, v30 offset1:1\n" \
  "ds_write_b32 v18, v31\n" \
  "v_mfma_f32_32x32x16_bf16 v[32:47], v[208:211], v[64:67], v[32:47]\n" \
  "s_waitcnt lgkmcnt(8)\n" \
  "v_mfma_f32_32x32x16_bf16 v[32:47], v[212:215], v[68:71], v[32:47]\n" \
  "s_waitcnt lgkmcnt(0)\n" \
  "s_barrier\n" \
  "ds_read_b64 v[80:81], v17\n" \
  "ds_read_b128 v[84:87], v19\n" \
  "v_mfma_f32_32x32x16_bf16 v[32:47], v[216:219], v[72:75], v[32:47]\n" \
  "v_mfma_f32_32x32x16_bf16 v[32:47], v[220:223], v[76:79], v[32:47]\n" \
  "s_waitcnt lgkmcnt(1)\n" \
  "v_add_f32 v29, v29, v80\n" \
  "v_add_f32 v30, v30, v81\n" \
  "v_mov_b32 v80, v29\n" \
  "v_mov_b32 v81, v30\n" \
  "s_waitcnt lgkmcnt(0)\n" \
  "v_add_f32 v84, v84, v85\n" \
  "v_add_f32 v86, v86, v87\n" \
  "v_permlane32_swap_b32 v29, v80\n" \
  "v_permlane32_swap_b32 v30, v81\n" \
  "v_add_f32 v29, v29, v80\n" \
  "v_add_f32 v30, v30, v81\n" \
  "v_mul_f32 v29, 0xbb000000, v29\n" \
  "v_add_f32 v31, v84, v86\n" \
  "v_cvt_pk_bf16_f32 v85, v29, v29\n" \
  "v_cvt_pk_bf16_f32 v48, v31, v31\n" \
  "v_and_b32 v85, 0xffff0000, v85\n" \
  "v_and_b32 v84, 0xffff0000, v48\n" \
  "v_sub_f32 v85, v29, v85\n" \
  "v_sub_f32 v84, v31, v84\n" \
  "v_cvt_pk_bf16_f32 v16, v29, v85\n" \
  "v_cvt_pk_bf16_f32 v49, v84, v84\n" \
  "v_cvt_pk_bf16_f32 v17, v29, v85\n" \
    \
  "v_mul_f32 v30, 0x3b000000, v30\n" \
  "v_fma_f32 v30, -v29, v29, v30\n" \
  "v_mfma_f32_32x32x8_bf16 v[32:47], v[16:17], v[48:49], v[32:47]\n" \
  "v_max_f32 v30, 0, v30\n" \
  "v_add_f32 v30, 0x3727c5ac, v30\n" \
  "v_rsq_f32 v30, v30\n" \
    \
  "v_mov_b32 v52, 0x3e803e80\n" \
  "v_mov_b32 v53, 0x3e803e80\n" \
  "v_cvt_pk_bf16_f32 v84, v30, v30\n" \
  "v_and_b32 v84, 0xffff0000, v84\n" \
  "v_sub_f32 v84, v30, v84\n" \
  "v_cvt_pk_bf16_f32 v50, v30, v84\n" \
  "v_cvt_pk_bf16_f32 v51, v30, v84\n" \
  "s_nop 1\n" \
  "v_mfma_f32_32x32x8_bf16 v[80:95], v[50:51], v[52:53], 0\n" \
  "s_nop 11\n" \
  "v_pk_mul_f32 v[32:33], v[32:33], v[80:81]\n" \
  "v_pk_mul_f32 v[34:35], v[34:35], v[82:83]\n" \
  "v_pk_mul_f32 v[36:37], v[36:37], v[84:85]\n" \
  "v_pk_mul_f32 v[38:39], v[38:39], v[86:87]\n" \
  "v_pk_mul_f32 v[40:41], v[40:41], v[88:89]\n" \
  "v_pk_mul_f32 v[42:43], v[42:43], v[90:91]\n" \
  "v_pk_mul_f32 v[44:45], v[44:45], v[92:93]\n" \
  "v_pk_mul_f32 v[46:47], v[46:47], v[94:95]\n" \
  "fbk_a3:\n" \
  "v_cvt_pk_bf16_f32 v80, v32, v33\n" \
  "v_cvt_pk_bf16_f32 v81, v34, v35\n" \
  "v_cvt_pk_bf16_f32 v82, v36, v37\n" \
  "v_cvt_pk_bf16_f32 v83, v38, v39\n" \
  "buffer_store_dwordx4 v[80:83], v20, s[12:15], s55 offen sc1\n" \
  "v_cvt_pk_bf16_f32 v84, v40, v41\n" \
  "v_cvt_pk_bf16_f32 v85, v42, v43\n" \
  "v_cvt_pk_bf16_f32 v86, v44, v45\n" \
  "v_cvt_pk_bf16_f32 v87, v46, v47\n" \
  "buffer_store_dwordx4 v[84:87], v21, s[12:15], s55 offen sc1\n" \
    \
  "s_waitcnt vmcnt(2) lgkmcnt(0)\n" \
  "s_barrier\n" \
  "s_cmp_lt_u32 s36, 4\n" \
  "s_cbranch_scc1 fbk_aq\n" \
    \
  "s_setprio 1\n" \
  "ds_read_b32 v253, v253\n" \
  "v_mov_b32 v31, 0\n" \
  "v_mov_b32 v27, 0\n" \
  "ds_read_b64_tr_b16 v[48:49], v12 offset:0\n" \
  "ds_read_b64_tr_b16 v[50:51], v12 offset:256\n" \
  "ds_read_b64_tr_b16 v[52:53], v12 offset:4096\n" \
  "ds_read_b64_tr_b16 v[54:55], v12 offset:4352\n" \
  "ds_read_b64_tr_b16 v[56:57], v12 offset:8192\n" \
  "ds_read_b64_tr_b16 v[58:59], v12 offset:8448\n" \
  "ds_read_b64_tr_b16 v[60:61], v12 offset:12288\n" \
  "ds_read_b64_tr_b16 v[62:63], v12 offset:12544\n" \
  "ds_read_b64_tr_b16 v[64:65], v12 offset:16384\n" \
  "ds_read_b64_tr_b16 v[66:67], v12 offset:16640\n" \
  "ds_read_b64_tr_b16 v[68:69], v12 offset:20480\n" \
  "ds_read_b64_tr_b16 v[70:71], v12 offset:20736\n" \
  "ds_read_b64_tr_b16 v[72:73], v12 offset:24576\n" \
  "ds_read_b64_tr_b16 v[74:75], v12 offset:24832\n" \
  "ds_read_b64_tr_b16 v[76:77], v12 offset:28672\n" \
  "ds_read_b64_tr_b16 v[78:79], v12 offset:28928\n" \
  "s_waitcnt lgkmcnt(0)\n" \
  "v_dot2c_f32_bf16 v31, 0x3f803f80, v48\n" \
  "v_dot2c_f32_bf16 v27, 0x3f803f80, v49\n" \
  "v_dot2c_f32_bf16 v31, 0x3f803f80, v50\n" \
  "v_dot2c_f32_bf16 v27, 0x3f803f80, v51\n" \
  "v_dot2c_f32_bf16 v31, 0x3f803f80, v52\n" \
  "v_dot2c_f32_bf16 v27, 0x3f803f80, v53\n" \
  "v_dot2c_f32_bf16 v31, 0x3f803f80, v54\n" \
  "v_dot2c_f32_bf16 v27, 0x3f803f80, v55\n" \
  "v_dot2c_f32_bf16 v31, 0x3f803f80, v56\n" \
  "v_dot2c_f32_bf16 v27, 0x3f803f80, v57\n" \
  "v_dot2c_f32_bf16 v31, 0x3f803f80, v58\n" \
  "v_dot2c_f32_bf16 v27, 0x3f803f80, v59\n" \
  "v_dot2c_f32_bf16 v31, 0x3f803f80, v60\n" \
  "v_dot2c_f32_bf16 v27, 0x3f803f80, v61\n" \
  "v_dot2c_f32_bf16 v31, 0x3f803f80, v62\n" \
  "v_dot2c_f32_bf16 v27, 0x3f803f80, v63\n" \
  "v_dot2c_f32_bf16 v31, 0x3f803f80, v64\n" \
  "v_dot2c_f32_bf16 v27, 0x3f803f80, v65\n" \
  "v_dot2c_f32_bf16 v31, 0x3f803f80, v66\n" \
  "v_dot2c_f32_bf16 v27, 0x3f803f80, v67\n" \
  "v_dot2c_f32_bf16 v31, 0x3f803f80, v68\n" \
  "v_dot2c_f32_bf16 v27, 0x3f803f80, v69\n" \
  "v_dot2c_f32_bf16 v31, 0x3f803f80, v70\n" \
  "v_dot2c_f32_bf16 v27, 0x3f803f80, v71\n" \
  "v_dot2c_f32_bf16 v31, 0x3f803f80, v72\n" \
  "v_dot2c_f32_bf16 v27, 0x3f803f80, v73\n" \
  "v_dot2c_f32_bf16 v31, 0x3f803f80, v74\n" \
  "v_dot2c_f32_bf16 v27, 0x3f803f80, v75\n" \
  "v_dot2c_f32_bf16 v31, 0x3f803f80, v76\n" \
  "v_dot2c_f32_bf16 v27, 0x3f803f80, v77\n" \
  "v_dot2c_f32_bf16 v31, 0x3f803f80, v78\n" \
  "v_dot2c_f32_bf16 v27, 0x3f803f80, v79\n" \
  "s_nop 2\n" \
  "v_add_f32 v31, v31, v27\n" \
  "ds_write_b32 v25, v31\n" \
  "s_setprio 0\n" \
  "s_cmp_eq_u32 s36, 7\n" \
  "s_cbranch_scc1 fbk_a9\n" \
  "s_waitcnt vmcnt(0) lgkmcnt(0)\n" \
  "s_barrier\n" \
  "s_branch fbk_a7\n" \
  "fbk_a9:\n" \
  "v_mov_b32 v29, 1\n" \
  "s_setprio 3\n" \
  "s_waitcnt vmcnt(0) lgkmcnt(0)\n" \
  "s_mov_b32 exec_lo, -1\n" \
  "s_mov_b32 exec_hi, 0\n" \
  "s_barrier\n" \
  "buffer_atomic_add v29, v249, s[12:15], 0 offen\n" \
  "s_mov_b64 exec, -1\n" \
  "fbk_a7:\n" \
    \
  "s_cmp_ge_u32 s36, 6\n" \
  "s_cbranch_scc1 fbk_a10\n" \
  "s_sub_u32 s47, s55, 262144\n" \
  "buffer_load_dword v24, v20, s[12:15], s55 offen sc1\n" \
  "buffer_load_dword v24, v21, s[12:15], s55 offen sc1\n" \
  "buffer_load_dword v24, v20, s[12:15], s47 offen sc1\n" \
  "buffer_load_dword v24, v21, s[12:15], s47 offen sc1\n" \
  "fbk_a10:\n" \
  "s_branch fbk_a4\n" \
  "fbk_aq:\n" \
    \
  "ds_read_b32 v253, v253\n" \
  "ds_read_b64_tr_b16 v[48:49], v12 offset:0\n" \
  "ds_read_b64_tr_b16 v[50:51], v12 offset:256\n" \
  "ds_read_b64_tr_b16 v[52:53], v13 offset:0\n" \
  "ds_read_b64_tr_b16 v[54:55], v13 offset:256\n" \
  "ds_read_b64_tr_b16 v[56:57], v14 offset:0\n" \
  "ds_read_b64_tr_b16 v[58:59], v14 offset:256\n" \
  "s_waitcnt lgkmcnt(4)\n" \
  "v_mfma_f32_32x32x16_bf16 v[32:47], v[48:51], v[96:99], 0\n" \
  "ds_read_b64_tr_b16 v[60:61], v15 offset:0\n" \
  "ds_read_b64_tr_b16 v[62:63], v15 offset:256\n" \
  "s_waitcnt lgkmcnt(4)\n" \
  "v_mfma_f32_32x32x16_bf16 v[32:47], v[52:55], v[100:103], v[32:47]\n" \
  "ds_read_b64_tr_b16 v[48:49], v12 offset:4096\n" \
  "ds_read_b64_tr_b16 v[50:51], v12 offset:4352\n" \
  "s_waitcnt lgkmcnt(4)\n" \
  "v_mfma_f32_32x32x16_bf16 v[32:47], v[56:59], v[104:107], v[32:47]\n" \
  "ds_read_b64_tr_b16 v[52:53], v13 offset:4096\n" \
  "ds_read_b64_tr_b16 v[54:55], v13 offset:4352\n" \
  "s_waitcnt lgkmcnt(4)\n" \
  "v_mfma_f32_32x32x16_bf16 v[32:47], v[60:63], v[108:111], v[32:47]\n" \
  "ds_read_b64_tr_b16 v[56:57], v14 offset:4096\n" \
  "ds_read_b64_tr_b16 v[58:59], v14 offset:4352\n" \
  "s_waitcnt lgkmcnt(4)\n" \
  "v_mfma_f32_32x32x16_bf16 v[32:47], v[48:51], v[112:115], v[32:47]\n" \
  "ds_read_b64_tr_b16 v[60:61], v15 offset:4096\n" \
  "ds_read_b64_tr_b16 v[62:63], v15 offset:4352\n" \
  "s_waitcnt lgkmcnt(4)\n" \
  "v_mfma_f32_32x32x16_bf16 v[32:47], v[52:55], v[116:119], v[32:47]\n" \
  "ds_read_b64_tr_b16 v[48:49], v12 offset:8192\n" \
  "ds_read_b64_tr_b16 v[50:51], v12 offset:8448\n" \
  "s_waitcnt lgkmcnt(4)\n" \
  "v_mfma_f32_32x32x16_bf16 v[32:47], v[56:59], v[120:123], v[32:47]\n" \
  "ds_read_b64_tr_b16 v[52:53], v13 offset:8192\n" \
  "ds_read_b64_tr_b16 v[54:55], v13 offset:8448\n" \
  "s_waitcnt lgkmcnt(4)\n" \
  "v_mfma_f32_32x32x16_bf16 v[32:47], v[60:63], v[124:127], v[32:47]\n" \
  "ds_read_b64_tr_b16 v[56:57], v14 offset:8192\n" \
  "ds_read_b64_tr_b16 v[58:59], v14 offset:8448\n" \
  "s_waitcnt lgkmcnt(4)\n" \
  "v_mfma_f32_32x32x16_bf16 v[32:47], v[48:51], v[128:131], v[32:47]\n" \
  "ds_read_b64_tr_b16 v[60:61], v15 offset:8192\n" \
  "ds_read_b64_tr_b16 v[62:63], v15 offset:8448\n" \
  "s_waitcnt lgkmcnt(4)\n" \
  "v_mfma_f32_32x32x16_bf16 v[32:47], v[52:55], v[132:135], v[32:47]\n" \
  "ds_read_b64_tr_b16 v[48:49], v12 offset:12288\n" \
  "ds_read_b64_tr_b16 v[50:51], v12 offset:12544\n" \
  "s_waitcnt lgkmcnt(4)\n" \
  "v_mfma_f32_32x32x16_bf16 v[32:47], v[56:59], v[136:139], v[32:47]\n" \
  "ds_read_b64_tr_b16 v[52:53], v13 offset:12288\n" \
  "ds_read_b64_tr_b16 v[54:55], v13 offset:12544\n" \
  "s_waitcnt lgkmcnt(4)\n" \
  "v_mfma_f32_32x32x16_bf16 v[32:47], v[60:63], v[140:143], v[32:47]\n" \
  "ds_read_b64_tr_b16 v[56:57], v14 offset:12288\n" \
  "ds_read_b64_tr_b16 v[58:59], v14 offset:12544\n" \
  "s_waitcnt lgkmcnt(4)\n" \
  "v_mfma_f32_32x32x16_bf16 v[32:47], v[48:51], v[144:147], v[32:47]\n" \
  "ds_read_b64_tr_b16 v[60:61], v15 offset:12288\n" \
  "ds_read_b64_tr_b16 v[62:63], v15 offset:12544\n" \
    \
  "s_waitcnt vmcnt(0)\n" \
  "s_barrier\n" \
  "ds_read_b128 v[88:91], v25\n" \
  "s_waitcnt lgkmcnt(5)\n" \
  "v_mfma_f32_32x32x16_bf16 v[32:47], v[52:55], v[148:151], v[32:47]\n" \
  "ds_read_b64_tr_b16 v[48:49], v12 offset:16384\n" \
  "ds_read_b64_tr_b16 v[50:51], v12 offset:16640\n" \
  "s_waitcnt lgkmcnt(5)\n" \
  "v_mfma_f32_32x32x16_bf16 v[32:47], v[56:59], v[152:155], v[32:47]\n" \
  "ds_read_b64_tr_b16 v[52:53], v13 offset:16384\n" \
  "ds_read_b64_tr_b16 v[54:55], v13 offset:16640\n" \
  "s_waitcnt lgkmcnt(5)\n" \
  "v_mfma_f32_32x32x16_bf16 v[32:47], v[60:63], v[156:159], v[32:47]\n" \
  "ds_read_b64_tr_b16 v[56:57], v14 offset:16384\n" \
  "ds_read_b64_tr_b16 v[58:59], v14 offset:16640\n" \
  "s_waitcnt lgkmcnt(4)\n" \
  "v_mfma_f32_32x32x16_bf16 v[32:47], v[48:51], v[160:163], v[32:47]\n" \
  "ds_read_b64_tr_b16 v[60:61], v15 offset:16384\n" \
  "ds_read_b64_tr_b16 v[62:63], v15 offset:16640\n" \
  "s_waitcnt lgkmcnt(4)\n" \
  "v_mfma_f32_32x32x16_bf16 v[32:47], v[52:55], v[164:167], v[32:47]\n" \
  "ds_read_b64_tr_b16 v[48:49], v12 offset:20480\n" \
  "ds_read_b64_tr_b16 v[50:51], v12 offset:20736\n" \
  "s_waitcnt lgkmcnt(4)\n" \
  "v_mfma_f32_32x32x16_bf16 v[32:47], v[56:59], v[168:171], v[32:47]\n" \
  "ds_read_b64_tr_b16 v[52:53], v13 offset:20480\n" \
  "ds_read_b64_tr_b16 v[54:55], v13 offset:20736\n" \
  "s_waitcnt lgkmcnt(4)\n" \
  "v_mfma_f32_32x32x16_bf16 v[32:47], v[60:63], v[172:175], v[32:47]\n" \
  "ds_read_b64_tr_b16 v[56:57], v14 offset:20480\n" \
  "ds_read_b64_tr_b16 v[58:59], v14 offset:20736\n" \
  "s_waitcnt lgkmcnt(4)\n" \
  "v_mfma_f32_32x32x16_bf16 v[32:47], v[48:51], v[176:179], v[32:47]\n" \
  "ds_read_b64_tr_b16 v[60:61], v15 offset:20480\n" \
  "ds_read_b64_tr_b16 v[62:63], v15 offset:20736\n" \
  "s_waitcnt lgkmcnt(4)\n" \
  "v_mfma_f32_32x32x16_bf16 v[32:47], v[52:55], v[180:183], v[32:47]\n" \
  "ds_read_b64_tr_b16 v[48:49], v12 offset:24576\n" \
  "ds_read_b64_tr_b16 v[50:51], v12 offset:24832\n" \
  "s_waitcnt lgkmcnt(4)\n" \
  "v_mfma_f32_32x32x16_bf16 v[32:47], v[56:59], v[184:187], v[32:47]\n" \
  "ds_read_b64_tr_b16 v[52:53], v13 offset:24576\n" \
  "ds_read_b64_tr_b16 v[54:55], v13 offset:24832\n" \
  "s_waitcnt lgkmcnt(4)\n" \
  "v_mfma_f32_32x32x16_bf16 v[32:47], v[60:63], v[188:191], v[32:47]\n" \
  "ds_read_b64_tr_b16 v[56:57], v14 offset:24576\n" \
  "ds_read_b64_tr_b16 v[58:59], v14 offset:24832\n" \
  "s_waitcnt lgkmcnt(4)\n" \
  "v_mfma_f32_32x32x16_bf16 v[32:47], v[48:51], v[192:195], v[32:47]\n" \
  "ds_read_b64_tr_b16 v[60:61], v15 offset:24576\n" \
  "ds_read_b64_tr_b16 v[62:63], v15 offset:24832\n" \
  "s_waitcnt lgkmcnt(4)\n" \
  "v_mfma_f32_32x32x16_bf16 v[32:47], v[52:55], v[196:199], v[32:47]\n" \
  "ds_read_b64_tr_b16 v[48:49], v12 offset:28672\n" \
  "ds_read_b64_tr_b16 v[50:51], v12 offset:28928\n" \
  "s_waitcnt lgkmcnt(4)\n" \
  "v_mfma_f32_32x32x16_bf16 v[32:47], v[56:59], v[200:203], v[32:47]\n" \
  "ds_read_b64_tr_b16 v[52:53], v13 offset:28672\n" \
  "ds_read_b64_tr_b16 v[54:55], v13 offset:28928\n" \
  "s_waitcnt lgkmcnt(4)\n" \
  "v_mfma_f32_32x32x16_bf16 v[32:47], v[60:63], v[204:207], v[32:47]\n" \
  "ds_read_b64_tr_b16 v[56:57], v14 offset:28672\n" \
  "ds_read_b64_tr_b16 v[58:59], v14 offset:28928\n" \
  "s_waitcnt lgkmcnt(4)\n" \
  "v_mfma_f32_32x32x16_bf16 v[32:47], v[48:51], v[208:211], v[32:47]\n" \
  "ds_read_b64_tr_b16 v[60:61], v15 offset:28672\n" \
  "ds_read_b64_tr_b16 v[62:63], v15 offset:28928\n" \
  "v_add_f32 v88, v88, v89\n" \
  "v_add_f32 v90, v90, v91\n" \
  "s_waitcnt lgkmcnt(4)\n" \
  "v_mfma_f32_32x32x16_bf16 v[32:47], v[52:55], v[212:215], v[32:47]\n" \
  "v_add_f32 v31, v88, v90\n" \
    \
  "v_mul_f32 v30, 0x3eb8aa3b, v30\n" \
  "s_waitcnt lgkmcnt(2)\n" \
  "v_mfma_f32_32x32x16_bf16 v[32:47], v[56:59], v[216:219], v[32:47]\n" \
  "v_cvt_pk_bf16_f32 v64, v31, v31\n" \
  "v_and_b32 v88, 0xffff0000, v64\n" \
  "v_sub_f32 v88, v31, v88\n" \
  "v_cvt_pk_bf16_f32 v65, v88, v88\n" \
  "s_waitcnt lgkmcnt(0)\n" \
  "v_mfma_f32_32x32x16_bf16 v[32:47], v[60:63], v[220:223], v[32:47]\n" \
  "v_mfma_f32_32x32x8_bf16 v[32:47], v[64:65], v[16:17], v[32:47]\n" \
  "s_nop 11\n" \
  "v_pk_mul_f32 v[32:33], v[32:33], v[30:31] op_sel_hi:[1,0]\n" \
  "v_pk_mul_f32 v[34:35], v[34:35], v[30:31] op_sel_hi:[1,0]\n" \
  "v_pk_mul_f32 v[36:37], v[36:37], v[30:31] op_sel_hi:[1,0]\n" \
  "v_pk_mul_f32 v[38:39], v[38:39], v[30:31] op_sel_hi:[1,0]\n" \
  "v_pk_mul_f32 v[40:41], v[40:41], v[30:31] op_sel_hi:[1,0]\n" \
  "v_pk_mul_f32 v[42:43], v[42:43], v[30:31] op_sel_hi:[1,0]\n" \
  "v_pk_mul_f32 v[44:45], v[44:45], v[30:31] op_sel_hi:[1,0]\n" \
  "v_pk_mul_f32 v[46:47], v[46:47], v[30:31] op_sel_hi:[1,0]\n" \
  "v_cvt_pk_bf16_f32 v4, v32, v33\n" \
  "v_cvt_pk_bf16_f32 v5, v34, v35\n" \
  "v_cvt_pk_bf16_f32 v6, v36, v37\n" \
  "v_cvt_pk_bf16_f32 v7, v38, v39\n" \
  "v_cvt_pk_bf16_f32 v8, v40, v41\n" \
  "v_cvt_pk_bf16_f32 v9, v42, v43\n" \
  "v_cvt_pk_bf16_f32 v10, v44, v45\n" \
  "v_cvt_pk_bf16_f32 v11, v46, v47\n" \
  "ds_write_b128 v26, v[4:7]\n" \
  "ds_write_b128 v26, v[8:11] offset:4096\n" \
  "fbk_a4:\n" \
    \
  "v_mov_b32 v176, 0\n" \
  "v_mov_b32 v177, 0\n" \
  "v_mov_b32 v178, 0\n" \
  "v_mov_b32 v179, 0\n" \
  "v_mov_b32 v184, v248\n" \
  "v_mov_b32 v185, v248\n" \
  "v_mov_b32 v186, v248\n" \
  "v_mov_b32 v187, v248\n" \
  "v_mov_b32 v188, v248\n" \
  "v_mov_b32 v189, v248\n" \
  "v_mov_b32 v190, v248\n" \
  "v_mov_b32 v191, v248\n" \
  "v_mfma_f32_32x32x16_bf16 v[64:79], v[176:179], v[176:179], 0\n" \
  "v_mov_b32 v192, v248\n" \
  "v_mov_b32 v193, v248\n" \
  "v_mfma_f32_32x32x16_bf16 v[80:95], v[176:179], v[176:179], 0\n" \
  "v_mov_b32 v194, v248\n" \
  "v_mov_b32 v195, v248\n" \
  "v_mfma_f32_32x32x16_bf16 v[96:111], v[176:179], v[176:179], 0\n" \
  "v_mov_b32 v196, v248\n" \
  "v_mov_b32 v197, v248\n" \
  "v_mfma_f32_32x32x16_bf16 v[112:127], v[176:179], v[176:179], 0\n" \
  "v_mov_b32 v198, v248\n" \
  "v_mov_b32 v199, v248\n" \
  "v_mov_b32 v232, 0xdddddddd\n" \
  "s_cmp_eq_u32 s36, 7\n" \
  "s_cbranch_scc1 fbk_a5\n" \
  "s_waitcnt lgkmcnt(0)\n" \
  "s_barrier\n" \
  "s_branch fbk_a6\n" \
  "fbk_a5:\n" \
    \
  "s_mov_b32 exec_lo, s81\n" \
  "s_mov_b32 exec_hi, 0\n" \
  "s_mov_b32 s47, 0\n" \
  "v_mov_b32 v16, 0\n" \
  "v_mov_b32 v17, 0\n" \
  "v_mov_b32 v18, 0\n" \
  "buffer_atomic_add v16, v252, s[12:15], 0 offen sc0\n" \
  "s_sleep 4\n" \
  "buffer_atomic_add v17, v252, s[12:15], 0 offen sc0\n" \
  "s_sleep 4\n" \
  "buffer_atomic_add v18, v252, s[12:15], 0 offen sc0\n" \
  "s_waitcnt vmcnt(2)\n" \
  "fbk_p1_loop:\n" \
  "v_sub_u32 v16, v16, v253\n" \
  "v_cmp_gt_u32 vcc, 1, v16\n" \
  "s_cbranch_vccz fbk_p1_ok\n" \
  "v_mov_b32 v16, 0\n" \
  "buffer_atomic_add v16, v252, s[12:15], 0 offen sc0\n" \
  "s_waitcnt vmcnt(2)\n" \
  "v_sub_u32 v17, v17, v253\n" \
  "v_cmp_gt_u32 vcc, 1, v17\n" \
  "s_cbranch_vccz fbk_p1_ok\n" \
  "v_mov_b32 v17, 0\n" \
  "buffer_atomic_add v17, v252, s[12:15], 0 offen sc0\n" \
  "s_waitcnt vmcnt(2)\n" \
  "v_sub_u32 v18, v18, v253\n" \
  "v_cmp_gt_u32 vcc, 1, v18\n" \
  "s_cbranch_vccz fbk_p1_ok\n" \
  "v_mov_b32 v18, 0\n" \
  "buffer_atomic_add v18, v252, s[12:15], 0 offen sc0\n" \
  "s_waitcnt vmcnt(2)\n" \
  "s_add_u32 s47, s47, 1\n" \
  "s_cmp_lt_u32 s47, 0x80000\n" \
  "s_cbranch_scc1 fbk_p1_loop\n" \
  "fbk_p1_ok:\n" \
  "s_waitcnt lgkmcnt(0)\n" \
  "s_barrier\n" \
  "s_mov_b64 exec, -1\n" \
  "s_setprio 0\n" \
  "fbk_a6:\n" \
    \
  "buffer_load_dwordx4 v[176:179], v251, s[12:15], s56 offen offset:0 sc1\n" \
  "s_mov_b64 exec, s[76:77]\n" \
  "buffer_load_dwordx4 v[184:187], v233, s[12:15], s56 offen offset:0 sc1\n" \
  "buffer_load_dwordx4 v[188:191], v233, s[12:15], s56 offen offset:512 sc1\n" \
  "s_mov_b32 exec_lo, -1\n" \
  "s_mov_b32 exec_hi, -1\n" \
  "buffer_load_dwordx4 v[180:183], v251, s[12:15], s56 offen offset:2048 sc1\n" \
  "ds_read_b128 v[160:163], v244\n" \
  "ds_read_b128 v[164:167], v245\n" \
  "ds_read_b128 v[168:171], v246\n" \
  "ds_read_b128 v[172:175], v247\n" \
  "s_setprio 3\n" \
  "s_waitcnt vmcnt(3) lgkmcnt(0)\n" \
  "v_mfma_f32_32x32x16_bf16 v[128:143], v[176:179], v[160:163], 0\n" \
  "s_nop 11\n" \
    \
  "v_exp_f32 v128, v128\n" \
  "v_exp_f32 v129, v129\n" \
  "v_exp_f32 v130, v130\n" \
  "v_exp_f32 v131, v131\n" \
  "v_exp_f32 v132, v132\n" \
  "v_mfma_f32_32x32x16_bf16 v[144:159], v[176:179], v[164:167], 0\n" \
  "v_exp_f32 v133, v133\n" \
  "s_mov_b64 exec, s[76:77]\n" \
  "buffer_load_dwordx4 v[192:195], v233, s[12:15], s56 offen offset:2048 sc1\n" \
  "s_add_u32 m0, s58, 0\n" \
  "buffer_load_dwordx4 v[196:199], v233, s[12:15], s56 offen offset:2560 sc1\n" \
  "s_mov_b32 exec_lo, -1\n" \
  "buffer_load_dwordx4 v234, s[16:19], s59 offen lds\n" \
  "s_mov_b32 exec_hi, -1\n" \
  "s_add_u32 s59, s59, 22016\n" \
  "v_exp_f32 v134, v134\n" \
  "v_exp_f32 v135, v135\n" \
  "v_exp_f32 v136, v136\n" \
  "v_exp_f32 v137, v137\n" \
  "s_waitcnt vmcnt(4)\n" \
  "v_smfmac_f32_32x32x32_bf16 v[64:79], v[184:187], v[128:135], v232\n" \
  "v_exp_f32 v138, v138\n" \
  "v_exp_f32 v139, v139\n" \
  "v_exp_f32 v140, v140\n" \
  "v_exp_f32 v141, v141\n" \
  "v_exp_f32 v142, v142\n" \
  "v_exp_f32 v143, v143\n" \
  "v_exp_f32 v144, v144\n" \
  "v_exp_f32 v145, v145\n" \
  "v_smfmac_f32_32x32x32_bf16 v[64:79], v[188:191], v[136:143], v232\n" \
  "v_exp_f32 v146, v146\n" \
  "v_exp_f32 v147, v147\n" \
  "v_exp_f32 v148, v148\n" \
  "v_mfma_f32_32x32x16_bf16 v[128:143], v[176:179], v[168:171], 0\n" \
  "v_exp_f32 v149, v149\n" \
  "v_exp_f32 v150, v150\n" \
  "v_exp_f32 v151, v151\n" \
  "v_exp_f32 v152, v152\n" \
  "v_exp_f32 v153, v153\n" \
  "v_smfmac_f32_32x32x32_bf16 v[80:95], v[184:187], v[144:151], v232\n" \
  "v_exp_f32 v154, v154\n" \
  "v_exp_f32 v155, v155\n" \
  "v_exp_f32 v156, v156\n" \
  "v_exp_f32 v157, v157\n" \
  "v_exp_f32 v158, v158\n" \
  "v_exp_f32 v159, v159\n" \
  "v_exp_f32 v128, v128\n" \
  "v_exp_f32 v129, v129\n" \
  "v_smfmac_f32_32x32x32_bf16 v[80:95], v[188:191], v[152:159], v232\n" \
  "v_exp_f32 v130, v130\n" \
  "v_exp_f32 v131, v131\n" \
  "v_exp_f32 v132, v132\n" \
  "v_mfma_f32_32x32x16_bf16 v[144:159], v[176:179], v[172:175], 0\n" \
  "v_exp_f32 v133, v133\n" \
  "s_xor_b32 s56, s56, 4096\n" \
  "buffer_load_dwordx4 v[176:179], v251, s[12:15], s56 offen offset:0 sc1\n" \
  "buffer_load_dwordx4 v[0:3], v254, s[24:27], s72 offen\n" \
  "s_add_u32 s72, s72, 16384\n" \
  "buffer_load_dwordx4 v[200:203], v254, s[20:23], s60 offen\n" \
  "s_xor_b32 s60, s60, 8192\n" \
  "v_exp_f32 v134, v134\n" \
  "v_exp_f32 v135, v135\n" \
  "v_exp_f32 v136, v136\n" \
  "v_exp_f32 v137, v137\n" \
  "v_smfmac_f32_32x32x32_bf16 v[96:111], v[184:187], v[128:135], v232\n" \
  "v_exp_f32 v138, v138\n" \
  "v_exp_f32 v139, v139\n" \
  "v_exp_f32 v140, v140\n" \
  "v_exp_f32 v141, v141\n" \
  "v_exp_f32 v142, v142\n" \
  "v_exp_f32 v143, v143\n" \
  "v_exp_f32 v144, v144\n" \
  "v_exp_f32 v145, v145\n" \
  "v_smfmac_f32_32x32x32_bf16 v[96:111], v[188:191], v[136:143], v232\n" \
  "v_exp_f32 v146, v146\n" \
  "v_exp_f32 v147, v147\n" \
  "v_exp_f32 v148, v148\n" \
  "s_waitcnt vmcnt(6)\n" \
  "v_mfma_f32_32x32x16_bf16 v[128:143], v[180:183], v[160:163], 0\n" \
  "v_exp_f32 v149, v149\n" \
  "v_exp_f32 v150, v150\n" \
  "v_exp_f32 v151, v151\n" \
  "v_exp_f32 v152, v152\n" \
  "v_exp_f32 v153, v153\n" \
  "v_smfmac_f32_32x32x32_bf16 v[112:127], v[184:187], v[144:151], v232\n" \
  "v_exp_f32 v154, v154\n" \
  "v_exp_f32 v155, v155\n" \
  "v_exp_f32 v156, v156\n" \
  "v_exp_f32 v157, v157\n" \
  "v_exp_f32 v158, v158\n" \
  "v_exp_f32 v159, v159\n" \
    \
  "v_exp_f32 v128, v128\n" \
  "v_exp_f32 v129, v129\n" \
  "v_smfmac_f32_32x32x32_bf16 v[112:127], v[188:191], v[152:159], v232\n" \
  "v_exp_f32 v130, v130\n" \
  "v_exp_f32 v131, v131\n" \
  "v_exp_f32 v132, v132\n" \
  "v_mfma_f32_32x32x16_bf16 v[144:159], v[180:183], v[164:167], 0\n" \
  "v_exp_f32 v133, v133\n" \
  "s_mov_b64 exec, s[76:77]\n" \
  "buffer_load_dwordx4 v[184:187], v233, s[12:15], s56 offen offset:0 sc1\n" \
  "s_add_u32 m0, s58, 768\n" \
  "buffer_load_dwordx4 v[188:191], v233, s[12:15], s56 offen offset:512 sc1\n" \
  "s_mov_b32 exec_lo, -1\n" \
  "buffer_load_dwordx4 v234, s[16:19], s59 offen lds\n" \
  "s_mov_b32 exec_hi, -1\n" \
  "s_add_u32 s59, s59, 22016\n" \
  "v_exp_f32 v134, v134\n" \
  "v_exp_f32 v135, v135\n" \
  "v_exp_f32 v136, v136\n" \
  "v_exp_f32 v137, v137\n" \
  "s_waitcnt vmcnt(7)\n" \
  "v_smfmac_f32_32x32x32_bf16 v[64:79], v[192:195], v[128:135], v232\n" \
  "v_exp_f32 v138, v138\n" \
  "v_exp_f32 v139, v139\n" \
  "v_exp_f32 v140, v140\n" \
  "v_exp_f32 v141, v141\n" \
  "v_exp_f32 v142, v142\n" \
  "v_exp_f32 v143, v143\n" \
  "v_exp_f32 v144, v144\n" \
  "v_exp_f32 v145, v145\n" \
  "v_smfmac_f32_32x32x32_bf16 v[64:79], v[196:199], v[136:143], v232\n" \
  "v_exp_f32 v146, v146\n" \
  "v_exp_f32 v147, v147\n" \
  "v_exp_f32 v148, v148\n" \
  "v_mfma_f32_32x32x16_bf16 v[128:143], v[180:183], v[168:171], 0\n" \
  "v_exp_f32 v149, v149\n" \
  "v_exp_f32 v150, v150\n" \
  "v_exp_f32 v151, v151\n" \
  "v_exp_f32 v152, v152\n" \
  "v_exp_f32 v153, v153\n" \
  "v_smfmac_f32_32x32x32_bf16 v[80:95], v[192:195], v[144:151], v232\n" \
  "v_exp_f32 v154, v154\n" \
  "v_exp_f32 v155, v155\n" \
  "v_exp_f32 v156, v156\n" \
  "v_exp_f32 v157, v157\n" \
  "v_exp_f32 v158, v158\n" \
  "v_exp_f32 v159, v159\n" \
  "v_exp_f32 v128, v128\n" \
  "v_exp_f32 v129, v129\n" \
  "v_smfmac_f32_32x32x32_bf16 v[80:95], v[196:199], v[152:159], v232\n" \
  "v_exp_f32 v130, v130\n" \
  "v_exp_f32 v131, v131\n" \
  "v_exp_f32 v132, v132\n" \
  "v_mfma_f32_32x32x16_bf16 v[144:159], v[180:183], v[172:175], 0\n" \
  "v_exp_f32 v133, v133\n" \
    \
  "v_mov_b32 v245, 0\n" \
  "s_mov_b32 exec_hi, 0\n" \
  "buffer_atomic_add v245, v252, s[12:15], 0 offen sc0\n" \
  "s_mov_b32 exec_hi, -1\n" \
  "buffer_load_dwordx4 v[180:183], v251, s[12:15], s56 offen offset:2048 sc1\n" \
  "buffer_load_dwordx4 v[4:7], v254, s[24:27], s72 offen\n" \
  "s_add_u32 s72, s72, 16384\n" \
  "v_exp_f32 v134, v134\n" \
  "v_exp_f32 v135, v135\n" \
  "v_exp_f32 v136, v136\n" \
  "v_exp_f32 v137, v137\n" \
  "v_smfmac_f32_32x32x32_bf16 v[96:111], v[192:195], v[128:135], v232\n" \
  "v_exp_f32 v138, v138\n" \
  "v_exp_f32 v139, v139\n" \
  "v_exp_f32 v140, v140\n" \
  "v_exp_f32 v141, v141\n" \
  "v_exp_f32 v142, v142\n" \
  "v_exp_f32 v143, v143\n" \
  "v_exp_f32 v144, v144\n" \
  "v_exp_f32 v145, v145\n" \
  "v_smfmac_f32_32x32x32_bf16 v[96:111], v[196:199], v[136:143], v232\n" \
  "v_exp_f32 v146, v146\n" \
  "v_exp_f32 v147, v147\n" \
  "v_exp_f32 v148, v148\n" \
  "s_waitcnt vmcnt(8)\n" \
  "v_mfma_f32_32x32x16_bf16 v[128:143], v[176:179], v[160:163], 0\n" \
  "v_exp_f32 v149, v149\n" \
  "v_exp_f32 v150, v150\n" \
  "v_exp_f32 v151, v151\n" \
  "v_exp_f32 v152, v152\n" \
  "v_exp_f32 v153, v153\n" \
  "v_smfmac_f32_32x32x32_bf16 v[112:127], v[192:195], v[144:151], v232\n" \
  "v_exp_f32 v154, v154\n" \
  "v_exp_f32 v155, v155\n" \
  "v_exp_f32 v156, v156\n" \
  "v_exp_f32 v157, v157\n" \
  "v_exp_f32 v158, v158\n" \
  "v_exp_f32 v159, v159\n" \
    \
  "v_exp_f32 v128, v128\n" \
  "v_exp_f32 v129, v129\n" \
  "v_smfmac_f32_32x32x32_bf16 v[112:127], v[196:199], v[152:159], v232\n" \
  "v_exp_f32 v130, v130\n" \
  "v_exp_f32 v131, v131\n" \
  "v_exp_f32 v132, v132\n" \
  "v_mfma_f32_32x32x16_bf16 v[144:159], v[176:179], v[164:167], 0\n" \
  "v_exp_f32 v133, v133\n" \
  "s_mov_b64 exec, s[76:77]\n" \
  "buffer_load_dwordx4 v[192:195], v233, s[12:15], s56 offen offset:2048 sc1\n" \
  "s_add_u32 m0, s58, 1536\n" \
  "buffer_load_dwordx4 v[196:199], v233, s[12:15], s56 offen offset:2560 sc1\n" \
  "s_mov_b32 exec_lo, -1\n" \
  "buffer_load_dwordx4 v234, s[16:19], s59 offen lds\n" \
  "s_mov_b32 exec_hi, -1\n" \
  "s_add_u32 s59, s59, 22016\n" \
  "v_exp_f32 v134, v134\n" \
  "v_exp_f32 v135, v135\n" \
  "v_exp_f32 v136, v136\n" \
  "v_exp_f32 v137, v137\n" \
  "s_waitcnt vmcnt(7)\n" \
  "v_smfmac_f32_32x32x32_bf16 v[64:79], v[184:187], v[128:135], v232\n" \
  "v_exp_f32 v138, v138\n" \
  "v_exp_f32 v139, v139\n" \
  "v_exp_f32 v140, v140\n" \
  "v_exp_f32 v141, v141\n" \
  "v_exp_f32 v142, v142\n" \
  "v_exp_f32 v143, v143\n" \
  "v_exp_f32 v144, v144\n" \
  "v_exp_f32 v145, v145\n" \
  "v_smfmac_f32_32x32x32_bf16 v[64:79], v[188:191], v[136:143], v232\n" \
  "v_exp_f32 v146, v146\n" \
  "v_exp_f32 v147, v147\n" \
  "v_exp_f32 v148, v148\n" \
  "v_mfma_f32_32x32x16_bf16 v[128:143], v[176:179], v[168:171], 0\n" \
  "v_exp_f32 v149, v149\n" \
  "v_exp_f32 v150, v150\n" \
  "v_exp_f32 v151, v151\n" \
  "v_exp_f32 v152, v152\n" \
  "v_exp_f32 v153, v153\n" \
  "v_smfmac_f32_32x32x32_bf16 v[80:95], v[184:187], v[144:151], v232\n" \
  "v_exp_f32 v154, v154\n" \
  "v_exp_f32 v155, v155\n" \
  "v_exp_f32 v156, v156\n" \
  "v_exp_f32 v157, v157\n" \
  "v_exp_f32 v158, v158\n" \
  "v_exp_f32 v159, v159\n" \
  "v_exp_f32 v128, v128\n" \
  "v_exp_f32 v129, v129\n" \
  "v_smfmac_f32_32x32x32_bf16 v[80:95], v[188:191], v[152:159], v232\n" \
  "v_exp_f32 v130, v130\n" \
  "v_exp_f32 v131, v131\n" \
  "v_exp_f32 v132, v132\n" \
  "v_mfma_f32_32x32x16_bf16 v[144:159], v[176:179], v[172:175], 0\n" \
  "v_exp_f32 v133, v133\n" \
    \
  "s_waitcnt vmcnt(5)\n" \
  "v_cmp_eq_u32 vcc, v245, v253\n" \
  "s_cmp_lg_u32 vcc_lo, 0\n" \
  "s_cbranch_scc0 fbk_b3\n" \
    \
  "fbk_wait32:\n" \
  "s_mov_b32 exec_hi, 0\n" \
  "s_mov_b32 s47, 0\n" \
  "fbk_w1:\n" \
  "v_mov_b32 v245, 0\n" \
  "buffer_atomic_add v245, v252, s[12:15], 0 offen sc0\n" \
  "s_waitcnt vmcnt(0)\n" \
  "v_cmp_eq_u32 vcc, v245, v253\n" \
  "s_cbranch_vccz fbk_w2\n" \
  "s_add_u32 s47, s47, 1\n" \
  "s_cmp_lt_u32 s47, 0x100000\n" \
  "s_cbranch_scc1 fbk_w1\n" \
  "fbk_w2:\n" \
  "s_mov_b32 exec_hi, -1\n" \
  "fbk_b3:\n" \
  "s_cmp_lg_u32 s36, 7\n" \
  "s_cbranch_scc1 fbk_b4\n" \
    \
  "v_mov_b32 v248, 1\n" \
  "s_mov_b32 exec_lo, -1\n" \
  "s_mov_b32 exec_hi, 0xff\n" \
  "buffer_atomic_add v248, v249, s[12:15], 0 offen\n" \
  "s_mov_b64 exec, -1\n" \
  "fbk_b4:\n" \
    \
  "s_mov_b32 s36, s91\n" \
  "s_lshr_b32 s46, s91, 2\n" \
  "s_xor_b32 s56, s56, 36864\n" \
  "buffer_load_dwordx4 v[176:179], v251, s[12:15], s56 offen offset:0 sc1\n" \
  "buffer_load_dwordx4 v[8:11], v254, s[24:27], s72 offen\n" \
  "s_add_u32 s72, s72, 65536\n" \
  "buffer_load_dwordx4 v[204:207], v254, s[20:23], s60 offen\n" \
  "s_xor_b32 s60, s60, 24576\n" \
  "v_exp_f32 v134, v134\n" \
  "v_exp_f32 v135, v135\n" \
  "v_exp_f32 v136, v136\n" \
  "v_exp_f32 v137, v137\n" \
  "v_smfmac_f32_32x32x32_bf16 v[96:111], v[184:187], v[128:135], v232\n" \
  "v_exp_f32 v138, v138\n" \
  "v_exp_f32 v139, v139\n" \
  "v_exp_f32 v140, v140\n" \
  "v_exp_f32 v141, v141\n" \
  "v_exp_f32 v142, v142\n" \
  "v_exp_f32 v143, v143\n" \
  "v_exp_f32 v144, v144\n" \
  "v_exp_f32 v145, v145\n" \
  "v_smfmac_f32_32x32x32_bf16 v[96:111], v[188:191], v[136:143], v232\n" \
  "v_exp_f32 v146, v146\n" \
  "v_exp_f32 v147, v147\n" \
  "v_exp_f32 v148, v148\n" \
  "s_waitcnt vmcnt(7)\n" \
  "v_mfma_f32_32x32x16_bf16 v[128:143], v[180:183], v[160:163], 0\n" \
  "v_exp_f32 v149, v149\n" \
  "v_exp_f32 v150, v150\n" \
  "v_exp_f32 v151, v151\n" \
  "v_exp_f32 v152, v152\n" \
  "v_exp_f32 v153, v153\n" \
  "v_smfmac_f32_32x32x32_bf16 v[112:127], v[184:187], v[144:151], v232\n" \
  "v_exp_f32 v154, v154\n" \
  "v_exp_f32 v155, v155\n" \
  "v_exp_f32 v156, v156\n" \
  "v_exp_f32 v157, v157\n" \
  "v_exp_f32 v158, v158\n" \
  "v_exp_f32 v159, v159\n" \
    \
  "v_exp_f32 v128, v128\n" \
  "v_exp_f32 v129, v129\n" \
  "v_smfmac_f32_32x32x32_bf16 v[112:127], v[188:191], v[152:159], v232\n" \
  "v_exp_f32 v130, v130\n" \
  "v_exp_f32 v131, v131\n" \
  "v_exp_f32 v132, v132\n" \
  "v_mfma_f32_32x32x16_bf16 v[144:159], v[180:183], v[164:167], 0\n" \
  "v_exp_f32 v133, v133\n" \
  "s_mov_b64 exec, s[76:77]\n" \
  "buffer_load_dwordx4 v[184:187], v233, s[12:15], s56 offen offset:0 sc1\n" \
  "s_add_u32 m0, s58, 2304\n" \
  "buffer_load_dwordx4 v[188:191], v233, s[12:15], s56 offen offset:512 sc1\n" \
  "s_mov_b32 exec_lo, -1\n" \
  "buffer_load_dwordx4 v234, s[16:19], s59 offen lds\n" \
  "s_mov_b32 exec_hi, -1\n" \
  "s_add_u32 s59, s59, 22016\n" \
  "v_exp_f32 v134, v134\n" \
  "v_exp_f32 v135, v135\n" \
  "v_exp_f32 v136, v136\n" \
  "v_exp_f32 v137, v137\n" \
  "s_waitcnt vmcnt(7)\n" \
  "v_smfmac_f32_32x32x32_bf16 v[64:79], v[192:195], v[128:135], v232\n" \
  "v_exp_f32 v138, v138\n" \
  "v_exp_f32 v139, v139\n" \
  "v_exp_f32 v140, v140\n" \
  "v_exp_f32 v141, v141\n" \
  "v_exp_f32 v142, v142\n" \
  "v_exp_f32 v143, v143\n" \
  "v_exp_f32 v144, v144\n" \
  "v_exp_f32 v145, v145\n" \
  "v_smfmac_f32_32x32x32_bf16 v[64:79], v[196:199], v[136:143], v232\n" \
  "v_exp_f32 v146, v146\n" \
  "v_exp_f32 v147, v147\n" \
  "v_exp_f32 v148, v148\n" \
  "v_mfma_f32_32x32x16_bf16 v[128:143], v[180:183], v[168:171], 0\n" \
  "v_exp_f32 v149, v149\n" \
  "v_exp_f32 v150, v150\n" \
  "v_exp_f32 v151, v151\n" \
  "v_exp_f32 v152, v152\n" \
  "v_exp_f32 v153, v153\n" \
  "v_smfmac_f32_32x32x32_bf16 v[80:95], v[192:195], v[144:151], v232\n" \
  "v_exp_f32 v154, v154\n" \
  "v_exp_f32 v155, v155\n" \
  "v_exp_f32 v156, v156\n" \
  "v_exp_f32 v157, v157\n" \
  "v_exp_f32 v158, v158\n" \
  "v_exp_f32 v159, v159\n" \
  "v_exp_f32 v128, v128\n" \
  "v_exp_f32 v129, v129\n" \
  "v_smfmac_f32_32x32x32_bf16 v[80:95], v[196:199], v[152:159], v232\n" \
  "v_exp_f32 v130, v130\n" \
  "v_exp_f32 v131, v131\n" \
  "v_exp_f32 v132, v132\n" \
  "v_mfma_f32_32x32x16_bf16 v[144:159], v[180:183], v[172:175], 0\n" \
  "v_exp_f32 v133, v133\n" \
  "buffer_load_dwordx4 v[180:183], v251, s[12:15], s56 offen offset:2048 sc1\n" \
  "buffer_load_dwordx4 v[12:15], v254, s[24:27], s72 offen\n" \
  "s_add_u32 s72, s72, 16384\n" \
  "v_exp_f32 v134, v134\n" \
  "v_exp_f32 v135, v135\n" \
  "v_exp_f32 v136, v136\n" \
  "v_exp_f32 v137, v137\n" \
  "v_smfmac_f32_32x32x32_bf16 v[96:111], v[192:195], v[128:135], v232\n" \
  "v_exp_f32 v138, v138\n" \
  "v_exp_f32 v139, v139\n" \
  "v_exp_f32 v140, v140\n" \
  "v_exp_f32 v141, v141\n" \
  "v_exp_f32 v142, v142\n" \
  "v_exp_f32 v143, v143\n" \
  "v_exp_f32 v144, v144\n" \
  "v_exp_f32 v145, v145\n" \
  "v_smfmac_f32_32x32x32_bf16 v[96:111], v[196:199], v[136:143], v232\n" \
  "v_exp_f32 v146, v146\n" \
  "v_exp_f32 v147, v147\n" \
  "v_exp_f32 v148, v148\n" \
  "s_waitcnt vmcnt(7)\n" \
  "v_mfma_f32_32x32x16_bf16 v[128:143], v[176:179], v[160:163], 0\n" \
  "v_exp_f32 v149, v149\n" \
  "v_exp_f32 v150, v150\n" \
  "v_exp_f32 v151, v151\n" \
  "v_exp_f32 v152, v152\n" \
  "v_exp_f32 v153, v153\n" \
  "v_smfmac_f32_32x32x32_bf16 v[112:127], v[192:195], v[144:151], v232\n" \
  "v_exp_f32 v154, v154\n" \
  "v_exp_f32 v155, v155\n" \
  "v_exp_f32 v156, v156\n" \
  "v_exp_f32 v157, v157\n" \
  "v_exp_f32 v158, v158\n" \
  "v_exp_f32 v159, v159\n" \
    \
  "v_exp_f32 v128, v128\n" \
  "v_exp_f32 v129, v129\n" \
  "v_smfmac_f32_32x32x32_bf16 v[112:127], v[196:199], v[152:159], v232\n" \
  "v_exp_f32 v130, v130\n" \
  "v_exp_f32 v131, v131\n" \
  "v_exp_f32 v132, v132\n" \
  "v_mfma_f32_32x32x16_bf16 v[144:159], v[176:179], v[164:167], 0\n" \
  "v_exp_f32 v133, v133\n" \
  "s_mov_b64 exec, s[76:77]\n" \
  "buffer_load_dwordx4 v[192:195], v233, s[12:15], s56 offen offset:2048 sc1\n" \
  "s_add_u32 m0, s58, 3072\n" \
  "buffer_load_dwordx4 v[196:199], v233, s[12:15], s56 offen offset:2560 sc1\n" \
  "s_mov_b32 exec_lo, -1\n" \
  "buffer_load_dwordx4 v234, s[16:19], s59 offen lds\n" \
  "s_mov_b32 exec_hi, -1\n" \
  "s_add_u32 s59, s59, 22016\n" \
  "v_exp_f32 v134, v134\n" \
  "v_exp_f32 v135, v135\n" \
  "v_exp_f32 v136, v136\n" \
  "v_exp_f32 v137, v137\n" \
  "s_waitcnt vmcnt(6)\n" \
  "v_smfmac_f32_32x32x32_bf16 v[64:79], v[184:187], v[128:135], v232\n" \
  "v_exp_f32 v138, v138\n" \
  "v_exp_f32 v139, v139\n" \
  "v_exp_f32 v140, v140\n" \
  "v_exp_f32 v141, v141\n" \
  "v_exp_f32 v142, v142\n" \
  "v_exp_f32 v143, v143\n" \
  "v_exp_f32 v144, v144\n" \
  "v_exp_f32 v145, v145\n" \
  "v_smfmac_f32_32x32x32_bf16 v[64:79], v[188:191], v[136:143], v232\n" \
  "v_exp_f32 v146, v146\n" \
  "v_exp_f32 v147, v147\n" \
  "v_exp_f32 v148, v148\n" \
  "v_mfma_f32_32x32x16_bf16 v[128:143], v[176:179], v[168:171], 0\n" \
  "v_exp_f32 v149, v149\n" \
  "v_exp_f32 v150, v150\n" \
  "v_exp_f32 v151, v151\n" \
  "v_exp_f32 v152, v152\n" \
  "v_exp_f32 v153, v153\n" \
  "v_smfmac_f32_32x32x32_bf16 v[80:95], v[184:187], v[144:151], v232\n" \
  "v_exp_f32 v154, v154\n" \
  "v_exp_f32 v155, v155\n" \
  "v_exp_f32 v156, v156\n" \
  "v_exp_f32 v157, v157\n" \
  "v_exp_f32 v158, v158\n" \
  "v_exp_f32 v159, v159\n" \
  "v_exp_f32 v128, v128\n" \
  "v_exp_f32 v129, v129\n" \
  "v_smfmac_f32_32x32x32_bf16 v[80:95], v[188:191], v[152:159], v232\n" \
  "v_exp_f32 v130, v130\n" \
  "v_exp_f32 v131, v131\n" \
  "v_exp_f32 v132, v132\n" \
  "v_mfma_f32_32x32x16_bf16 v[144:159], v[176:179], v[172:175], 0\n" \
  "v_exp_f32 v133, v133\n" \
  "s_xor_b32 s56, s56, 4096\n" \
  "buffer_load_dwordx4 v[176:179], v251, s[12:15], s56 offen offset:0 sc1\n" \
  "buffer_load_dwordx4 v[16:19], v254, s[24:27], s72 offen\n" \
  "s_add_u32 s72, s72, 16384\n" \
  "buffer_load_dwordx4 v[208:211], v254, s[20:23], s60 offen\n" \
  "s_xor_b32 s60, s60, 8192\n" \
  "v_exp_f32 v134, v134\n" \
  "v_exp_f32 v135, v135\n" \
  "v_exp_f32 v136, v136\n" \
  "v_exp_f32 v137, v137\n" \
  "v_smfmac_f32_32x32x32_bf16 v[96:111], v[184:187], v[128:135], v232\n" \
  "v_exp_f32 v138, v138\n" \
  "v_exp_f32 v139, v139\n" \
  "v_exp_f32 v140, v140\n" \
  "v_exp_f32 v141, v141\n" \
  "v_exp_f32 v142, v142\n" \
  "v_exp_f32 v143, v143\n" \
  "v_exp_f32 v144, v144\n" \
  "v_exp_f32 v145, v145\n" \
  "v_smfmac_f32_32x32x32_bf16 v[96:111], v[188:191], v[136:143], v232\n" \
  "v_exp_f32 v146, v146\n" \
  "v_exp_f32 v147, v147\n" \
  "v_exp_f32 v148, v148\n" \
  "s_waitcnt vmcnt(7)\n" \
  "v_mfma_f32_32x32x16_bf16 v[128:143], v[180:183], v[160:163], 0\n" \
  "v_exp_f32 v149, v149\n" \
  "v_exp_f32 v150, v150\n" \
  "v_exp_f32 v151, v151\n" \
  "v_exp_f32 v152, v152\n" \
  "v_exp_f32 v153, v153\n" \
  "v_smfmac_f32_32x32x32_bf16 v[112:127], v[184:187], v[144:151], v232\n" \
  "v_exp_f32 v154, v154\n" \
  "v_exp_f32 v155, v155\n" \
  "v_exp_f32 v156, v156\n" \
  "v_exp_f32 v157, v157\n" \
  "v_exp_f32 v158, v158\n" \
  "v_exp_f32 v159, v159\n" \
    \
  "v_exp_f32 v128, v128\n" \
  "v_exp_f32 v129, v129\n" \
  "v_smfmac_f32_32x32x32_bf16 v[112:127], v[188:191], v[152:159], v232\n" \
  "v_exp_f32 v130, v130\n" \
  "v_exp_f32 v131, v131\n" \
  "v_exp_f32 v132, v132\n" \
  "v_mfma_f32_32x32x16_bf16 v[144:159], v[180:183], v[164:167], 0\n" \
  "v_exp_f32 v133, v133\n" \
  "s_mov_b64 exec, s[76:77]\n" \
  "buffer_load_dwordx4 v[184:187], v233, s[12:15], s56 offen offset:0 sc1\n" \
  "s_add_u32 m0, s58, 3840\n" \
  "buffer_load_dwordx4 v[188:191], v233, s[12:15], s56 offen offset:512 sc1\n" \
  "s_mov_b32 exec_lo, -1\n" \
  "buffer_load_dwordx4 v234, s[16:19], s59 offen lds\n" \
  "s_mov_b32 exec_hi, -1\n" \
  "s_add_u32 s59, s59, 22016\n" \
  "v_exp_f32 v134, v134\n" \
  "v_exp_f32 v135, v135\n" \
  "v_exp_f32 v136, v136\n" \
  "v_exp_f32 v137, v137\n" \
  "s_waitcnt vmcnt(7)\n" \
  "v_smfmac_f32_32x32x32_bf16 v[64:79], v[192:195], v[128:135], v232\n" \
  "v_exp_f32 v138, v138\n" \
  "v_exp_f32 v139, v139\n" \
  "v_exp_f32 v140, v140\n" \
  "v_exp_f32 v141, v141\n" \
  "v_exp_f32 v142, v142\n" \
  "v_exp_f32 v143, v143\n" \
  "v_exp_f32 v144, v144\n" \
  "v_exp_f32 v145, v145\n" \
  "v_smfmac_f32_32x32x32_bf16 v[64:79], v[196:199], v[136:143], v232\n" \
  "v_exp_f32 v146, v146\n" \
  "v_exp_f32 v147, v147\n" \
  "v_exp_f32 v148, v148\n" \
  "v_mfma_f32_32x32x16_bf16 v[128:143], v[180:183], v[168:171], 0\n" \
  "v_exp_f32 v149, v149\n" \
  "v_exp_f32 v150, v150\n" \
  "v_exp_f32 v151, v151\n" \
  "v_exp_f32 v152, v152\n" \
  "v_exp_f32 v153, v153\n" \
  "v_smfmac_f32_32x32x32_bf16 v[80:95], v[192:195], v[144:151], v232\n" \
  "v_exp_f32 v154, v154\n" \
  "v_exp_f32 v155, v155\n" \
  "v_exp_f32 v156, v156\n" \
  "v_exp_f32 v157, v157\n" \
  "v_exp_f32 v158, v158\n" \
  "v_exp_f32 v159, v159\n" \
  "v_exp_f32 v128, v128\n" \
  "v_exp_f32 v129, v129\n" \
  "v_smfmac_f32_32x32x32_bf16 v[80:95], v[196:199], v[152:159], v232\n" \
  "v_exp_f32 v130, v130\n" \
  "v_exp_f32 v131, v131\n" \
  "v_exp_f32 v132, v132\n" \
  "v_mfma_f32_32x32x16_bf16 v[144:159], v[180:183], v[172:175], 0\n" \
  "v_exp_f32 v133, v133\n" \
  "buffer_load_dwordx4 v[180:183], v251, s[12:15], s56 offen offset:2048 sc1\n" \
  "buffer_load_dwordx4 v[20:23], v254, s[24:27], s72 offen\n" \
  "s_add_u32 s72, s72, 65536\n" \
  "v_exp_f32 v134, v134\n" \
  "v_exp_f32 v135, v135\n" \
  "v_exp_f32 v136, v136\n" \
  "v_exp_f32 v137, v137\n" \
  "v_smfmac_f32_32x32x32_bf16 v[96:111], v[192:195], v[128:135], v232\n" \
  "v_exp_f32 v138, v138\n" \
  "v_exp_f32 v139, v139\n" \
  "v_exp_f32 v140, v140\n" \
  "v_exp_f32 v141, v141\n" \
  "v_exp_f32 v142, v142\n" \
  "v_exp_f32 v143, v143\n" \
  "v_exp_f32 v144, v144\n" \
  "v_exp_f32 v145, v145\n" \
  "v_smfmac_f32_32x32x32_bf16 v[96:111], v[196:199], v[136:143], v232\n" \
  "v_exp_f32 v146, v146\n" \
  "v_exp_f32 v147, v147\n" \
  "v_exp_f32 v148, v148\n" \
  "s_waitcnt vmcnt(7)\n" \
  "v_mfma_f32_32x32x16_bf16 v[128:143], v[176:179], v[160:163], 0\n" \
  "v_exp_f32 v149, v149\n" \
  "v_exp_f32 v150, v150\n" \
  "v_exp_f32 v151, v151\n" \
  "v_exp_f32 v152, v152\n" \
  "v_exp_f32 v153, v153\n" \
  "v_smfmac_f32_32x32x32_bf16 v[112:127], v[192:195], v[144:151], v232\n" \
  "v_exp_f32 v154, v154\n" \
  "v_exp_f32 v155, v155\n" \
  "v_exp_f32 v156, v156\n" \
  "v_exp_f32 v157, v157\n" \
  "v_exp_f32 v158, v158\n" \
  "v_exp_f32 v159, v159\n" \
    \
  "v_exp_f32 v128, v128\n" \
  "v_exp_f32 v129, v129\n" \
  "v_smfmac_f32_32x32x32_bf16 v[112:127], v[196:199], v[152:159], v232\n" \
  "v_exp_f32 v130, v130\n" \
  "v_exp_f32 v131, v131\n" \
  "v_exp_f32 v132, v132\n" \
  "v_mfma_f32_32x32x16_bf16 v[144:159], v[176:179], v[164:167], 0\n" \
  "v_exp_f32 v133, v133\n" \
  "s_mov_b64 exec, s[76:77]\n" \
  "buffer_load_dwordx4 v[192:195], v233, s[12:15], s56 offen offset:2048 sc1\n" \
  "s_add_u32 m0, s58, 4608\n" \
  "buffer_load_dwordx4 v[196:199], v233, s[12:15], s56 offen offset:2560 sc1\n" \
  "s_mov_b32 exec_lo, -1\n" \
  "buffer_load_dwordx4 v234, s[16:19], s59 offen lds\n" \
  "s_mov_b32 exec_hi, -1\n" \
  "s_add_u32 s59, s59, 22016\n" \
  "v_exp_f32 v134, v134\n" \
  "v_exp_f32 v135, v135\n" \
  "v_exp_f32 v136, v136\n" \
  "v_exp_f32 v137, v137\n" \
  "s_waitcnt vmcnt(6)\n" \
  "v_smfmac_f32_32x32x32_bf16 v[64:79], v[184:187], v[128:135], v232\n" \
  "v_exp_f32 v138, v138\n" \
  "v_exp_f32 v139, v139\n" \
  "v_exp_f32 v140, v140\n" \
  "v_exp_f32 v141, v141\n" \
  "v_exp_f32 v142, v142\n" \
  "v_exp_f32 v143, v143\n" \
  "v_exp_f32 v144, v144\n" \
  "v_exp_f32 v145, v145\n" \
  "v_smfmac_f32_32x32x32_bf16 v[64:79], v[188:191], v[136:143], v232\n" \
  "v_exp_f32 v146, v146\n" \
  "v_exp_f32 v147, v147\n" \
  "v_exp_f32 v148, v148\n" \
  "v_mfma_f32_32x32x16_bf16 v[128:143], v[176:179], v[168:171], 0\n" \
  "v_exp_f32 v149, v149\n" \
  "v_exp_f32 v150, v150\n" \
  "v_exp_f32 v151, v151\n" \
  "v_exp_f32 v152, v152\n" \
  "v_exp_f32 v153, v153\n" \
  "v_smfmac_f32_32x32x32_bf16 v[80:95], v[184:187], v[144:151], v232\n" \
  "v_exp_f32 v154, v154\n" \
  "v_exp_f32 v155, v155\n" \
  "v_exp_f32 v156, v156\n" \
  "v_exp_f32 v157, v157\n" \
  "v_exp_f32 v158, v158\n" \
  "v_exp_f32 v159, v159\n" \
  "v_exp_f32 v128, v128\n" \
  "v_exp_f32 v129, v129\n" \
  "v_smfmac_f32_32x32x32_bf16 v[80:95], v[188:191], v[152:159], v232\n" \
  "v_exp_f32 v130, v130\n" \
  "v_exp_f32 v131, v131\n" \
  "v_exp_f32 v132, v132\n" \
  "v_mfma_f32_32x32x16_bf16 v[144:159], v[176:179], v[172:175], 0\n" \
  "v_exp_f32 v133, v133\n" \
  "s_xor_b32 s56, s56, 102400\n" \
  "buffer_load_dwordx4 v[176:179], v251, s[12:15], s56 offen offset:0 sc1\n" \
  "buffer_load_dwordx4 v[24:27], v254, s[24:27], s72 offen\n" \
  "s_add_u32 s72, s72, 16384\n" \
  "buffer_load_dwordx4 v[212:215], v254, s[20:23], s60 offen\n" \
  "s_xor_b32 s60, s60, 57344\n" \
  "v_exp_f32 v134, v134\n" \
  "v_exp_f32 v135, v135\n" \
  "v_exp_f32 v136, v136\n" \
  "v_exp_f32 v137, v137\n" \
  "v_smfmac_f32_32x32x32_bf16 v[96:111], v[184:187], v[128:135], v232\n" \
  "v_exp_f32 v138, v138\n" \
  "v_exp_f32 v139, v139\n" \
  "v_exp_f32 v140, v140\n" \
  "v_exp_f32 v141, v141\n" \
  "v_exp_f32 v142, v142\n" \
  "v_exp_f32 v143, v143\n" \
  "v_exp_f32 v144, v144\n" \
  "v_exp_f32 v145, v145\n" \
  "v_smfmac_f32_32x32x32_bf16 v[96:111], v[188:191], v[136:143], v232\n" \
  "v_exp_f32 v146, v146\n" \
  "v_exp_f32 v147, v147\n" \
  "v_exp_f32 v148, v148\n" \
  "s_waitcnt vmcnt(7)\n" \
  "v_mfma_f32_32x32x16_bf16 v[128:143], v[180:183], v[160:163], 0\n" \
  "v_exp_f32 v149, v149\n" \
  "v_exp_f32 v150, v150\n" \
  "v_exp_f32 v151, v151\n" \
  "v_exp_f32 v152, v152\n" \
  "v_exp_f32 v153, v153\n" \
  "v_smfmac_f32_32x32x32_bf16 v[112:127], v[184:187], v[144:151], v232\n" \
  "v_exp_f32 v154, v154\n" \
  "v_exp_f32 v155, v155\n" \
  "v_exp_f32 v156, v156\n" \
  "v_exp_f32 v157, v157\n" \
  "v_exp_f32 v158, v158\n" \
  "v_exp_f32 v159, v159\n" \
    \
  "v_exp_f32 v128, v128\n" \
  "v_exp_f32 v129, v129\n" \
  "v_smfmac_f32_32x32x32_bf16 v[112:127], v[188:191], v[152:159], v232\n" \
  "v_exp_f32 v130, v130\n" \
  "v_exp_f32 v131, v131\n" \
  "v_exp_f32 v132, v132\n" \
  "v_mfma_f32_32x32x16_bf16 v[144:159], v[180:183], v[164:167], 0\n" \
  "v_exp_f32 v133, v133\n" \
  "s_mov_b64 exec, s[76:77]\n" \
  "buffer_load_dwordx4 v[184:187], v233, s[12:15], s56 offen offset:0 sc1\n" \
  "s_add_u32 m0, s58, 5376\n" \
  "buffer_load_dwordx4 v[188:191], v233, s[12:15], s56 offen offset:512 sc1\n" \
  "s_mov_b32 exec_lo, -1\n" \
  "buffer_load_dwordx4 v234, s[16:19], s59 offen lds\n" \
  "s_mov_b32 exec_hi, -1\n" \
  "s_add_u32 s59, s59, 22016\n" \
  "v_exp_f32 v134, v134\n" \
  "v_exp_f32 v135, v135\n" \
  "v_exp_f32 v136, v136\n" \
  "v_exp_f32 v137, v137\n" \
  "s_waitcnt vmcnt(7)\n" \
  "v_smfmac_f32_32x32x32_bf16 v[64:79], v[192:195], v[128:135], v232\n" \
  "v_exp_f32 v138, v138\n" \
  "v_exp_f32 v139, v139\n" \
  "v_exp_f32 v140, v140\n" \
  "v_exp_f32 v141, v141\n" \
  "v_exp_f32 v142, v142\n" \
  "v_exp_f32 v143, v143\n" \
  "v_exp_f32 v144, v144\n" \
  "v_exp_f32 v145, v145\n" \
  "v_smfmac_f32_32x32x32_bf16 v[64:79], v[196:199], v[136:143], v232\n" \
  "v_exp_f32 v146, v146\n" \
  "v_exp_f32 v147, v147\n" \
  "v_exp_f32 v148, v148\n" \
  "v_mfma_f32_32x32x16_bf16 v[128:143], v[180:183], v[168:171], 0\n" \
  "v_exp_f32 v149, v149\n" \
  "v_exp_f32 v150, v150\n" \
  "v_exp_f32 v151, v151\n" \
  "v_exp_f32 v152, v152\n" \
  "v_exp_f32 v153, v153\n" \
  "v_smfmac_f32_32x32x32_bf16 v[80:95], v[192:195], v[144:151], v232\n" \
  "v_exp_f32 v154, v154\n" \
  "v_exp_f32 v155, v155\n" \
  "v_exp_f32 v156, v156\n" \
  "v_exp_f32 v157, v157\n" \
  "v_exp_f32 v158, v158\n" \
  "v_exp_f32 v159, v159\n" \
  "v_exp_f32 v128, v128\n" \
  "v_exp_f32 v129, v129\n" \
  "v_smfmac_f32_32x32x32_bf16 v[80:95], v[196:199], v[152:159], v232\n" \
  "v_exp_f32 v130, v130\n" \
  "v_exp_f32 v131, v131\n" \
  "v_exp_f32 v132, v132\n" \
  "v_mfma_f32_32x32x16_bf16 v[144:159], v[180:183], v[172:175], 0\n" \
  "v_exp_f32 v133, v133\n" \
  "buffer_load_dwordx4 v[180:183], v251, s[12:15], s56 offen offset:2048 sc1\n" \
  "buffer_load_dwordx4 v[28:31], v254, s[24:27], s72 offen\n" \
  "s_add_u32 s72, s72, 16384\n" \
  "v_exp_f32 v134, v134\n" \
  "v_exp_f32 v135, v135\n" \
  "v_exp_f32 v136, v136\n" \
  "v_exp_f32 v137, v137\n" \
  "v_smfmac_f32_32x32x32_bf16 v[96:111], v[192:195], v[128:135], v232\n" \
  "v_exp_f32 v138, v138\n" \
  "v_exp_f32 v139, v139\n" \
  "v_exp_f32 v140, v140\n" \
  "v_exp_f32 v141, v141\n" \
  "v_exp_f32 v142, v142\n" \
  "v_exp_f32 v143, v143\n" \
  "v_exp_f32 v144, v144\n" \
  "v_exp_f32 v145, v145\n" \
  "v_smfmac_f32_32x32x32_bf16 v[96:111], v[196:199], v[136:143], v232\n" \
  "v_exp_f32 v146, v146\n" \
  "v_exp_f32 v147, v147\n" \
  "v_exp_f32 v148, v148\n" \
  "s_waitcnt vmcnt(7)\n" \
  "v_mfma_f32_32x32x16_bf16 v[128:143], v[176:179], v[160:163], 0\n" \
  "v_exp_f32 v149, v149\n" \
  "v_exp_f32 v150, v150\n" \
  "v_exp_f32 v151, v151\n" \
  "v_exp_f32 v152, v152\n" \
  "v_exp_f32 v153, v153\n" \
  "v_smfmac_f32_32x32x32_bf16 v[112:127], v[192:195], v[144:151], v232\n" \
  "v_exp_f32 v154, v154\n" \
  "v_exp_f32 v155, v155\n" \
  "v_exp_f32 v156, v156\n" \
  "v_exp_f32 v157, v157\n" \
  "v_exp_f32 v158, v158\n" \
  "v_exp_f32 v159, v159\n" \
    \
  "v_exp_f32 v128, v128\n" \
  "v_exp_f32 v129, v129\n" \
  "v_smfmac_f32_32x32x32_bf16 v[112:127], v[196:199], v[152:159], v232\n" \
  "v_exp_f32 v130, v130\n" \
  "v_exp_f32 v131, v131\n" \
  "v_exp_f32 v132, v132\n" \
  "v_mfma_f32_32x32x16_bf16 v[144:159], v[176:179], v[164:167], 0\n" \
  "v_exp_f32 v133, v133\n" \
  "s_mov_b64 exec, s[76:77]\n" \
  "buffer_load_dwordx4 v[192:195], v233, s[12:15], s56 offen offset:2048 sc1\n" \
  "s_add_u32 m0, s58, 6144\n" \
  "buffer_load_dwordx4 v[196:199], v233, s[12:15], s56 offen offset:2560 sc1\n" \
  "s_mov_b32 exec_lo, -1\n" \
  "buffer_load_dwordx4 v234, s[16:19], s59 offen lds\n" \
  "s_mov_b32 exec_hi, -1\n" \
  "s_add_u32 s59, s59, 22016\n" \
  "v_exp_f32 v134, v134\n" \
  "v_exp_f32 v135, v135\n" \
  "v_exp_f32 v136, v136\n" \
  "v_exp_f32 v137, v137\n" \
  "s_waitcnt vmcnt(6)\n" \
  "v_smfmac_f32_32x32x32_bf16 v[64:79], v[184:187], v[128:135], v232\n" \
  "v_exp_f32 v138, v138\n" \
  "v_exp_f32 v139, v139\n" \
  "v_exp_f32 v140, v140\n" \
  "v_exp_f32 v141, v141\n" \
  "v_exp_f32 v142, v142\n" \
  "v_exp_f32 v143, v143\n" \
  "v_exp_f32 v144, v144\n" \
  "v_exp_f32 v145, v145\n" \
  "v_smfmac_f32_32x32x32_bf16 v[64:79], v[188:191], v[136:143], v232\n" \
  "v_exp_f32 v146, v146\n" \
  "v_exp_f32 v147, v147\n" \
  "v_exp_f32 v148, v148\n" \
  "v_mfma_f32_32x32x16_bf16 v[128:143], v[176:179], v[168:171], 0\n" \
  "v_exp_f32 v149, v149\n" \
  "v_exp_f32 v150, v150\n" \
  "v_exp_f32 v151, v151\n" \
  "v_exp_f32 v152, v152\n" \
  "v_exp_f32 v153, v153\n" \
  "v_smfmac_f32_32x32x32_bf16 v[80:95], v[184:187], v[144:151], v232\n" \
  "v_exp_f32 v154, v154\n" \
  "v_exp_f32 v155, v155\n" \
  "v_exp_f32 v156, v156\n" \
  "v_exp_f32 v157, v157\n" \
  "v_exp_f32 v158, v158\n" \
  "v_exp_f32 v159, v159\n" \
  "v_exp_f32 v128, v128\n" \
  "v_exp_f32 v129, v129\n" \
  "v_smfmac_f32_32x32x32_bf16 v[80:95], v[188:191], v[152:159], v232\n" \
  "v_exp_f32 v130, v130\n" \
  "v_exp_f32 v131, v131\n" \
  "v_exp_f32 v132, v132\n" \
  "v_mfma_f32_32x32x16_bf16 v[144:159], v[176:179], v[172:175], 0\n" \
  "v_exp_f32 v133, v133\n" \
  "s_xor_b32 s56, s56, 4096\n" \
  "buffer_load_dwordx4 v[176:179], v251, s[12:15], s56 offen offset:0 sc1\n" \
  "buffer_load_dwordx4 v[32:35], v254, s[24:27], s72 offen\n" \
  "s_add_u32 s72, s72, 65536\n" \
  "buffer_load_dwordx4 v[216:219], v254, s[20:23], s60 offen\n" \
  "s_xor_b32 s60, s60, 8192\n" \
  "v_exp_f32 v134, v134\n" \
  "v_exp_f32 v135, v135\n" \
  "v_exp_f32 v136, v136\n" \
  "v_exp_f32 v137, v137\n" \
  "v_smfmac_f32_32x32x32_bf16 v[96:111], v[184:187], v[128:135], v232\n" \
  "v_exp_f32 v138, v138\n" \
  "v_exp_f32 v139, v139\n" \
  "v_exp_f32 v140, v140\n" \
  "v_exp_f32 v141, v141\n" \
  "v_exp_f32 v142, v142\n" \
  "v_exp_f32 v143, v143\n" \
  "v_exp_f32 v144, v144\n" \
  "v_exp_f32 v145, v145\n" \
  "v_smfmac_f32_32x32x32_bf16 v[96:111], v[188:191], v[136:143], v232\n" \
  "v_exp_f32 v146, v146\n" \
  "v_exp_f32 v147, v147\n" \
  "v_exp_f32 v148, v148\n" \
  "s_waitcnt vmcnt(7)\n" \
  "v_mfma_f32_32x32x16_bf16 v[128:143], v[180:183], v[160:163], 0\n" \
  "v_exp_f32 v149, v149\n" \
  "v_exp_f32 v150, v150\n" \
  "v_exp_f32 v151, v151\n" \
  "v_exp_f32 v152, v152\n" \
  "v_exp_f32 v153, v153\n" \
  "v_smfmac_f32_32x32x32_bf16 v[112:127], v[184:187], v[144:151], v232\n" \
  "v_exp_f32 v154, v154\n" \
  "v_exp_f32 v155, v155\n" \
  "v_exp_f32 v156, v156\n" \
  "v_exp_f32 v157, v157\n" \
  "v_exp_f32 v158, v158\n" \
  "v_exp_f32 v159, v159\n" \
    \
  "s_setprio 2\n" \
  "v_exp_f32 v128, v128\n" \
  "v_exp_f32 v129, v129\n" \
  "v_smfmac_f32_32x32x32_bf16 v[112:127], v[188:191], v[152:159], v232\n" \
  "v_exp_f32 v130, v130\n" \
  "v_exp_f32 v131, v131\n" \
  "v_exp_f32 v132, v132\n" \
  "v_mfma_f32_32x32x16_bf16 v[144:159], v[180:183], v[164:167], 0\n" \
  "v_exp_f32 v133, v133\n" \
  "s_mov_b64 exec, s[76:77]\n" \
  "buffer_load_dwordx4 v[184:187], v233, s[12:15], s56 offen offset:0 sc1\n" \
  "s_add_u32 m0, s58, 6912\n" \
  "buffer_load_dwordx4 v[188:191], v233, s[12:15], s56 offen offset:512 sc1\n" \
  "s_mov_b32 exec_lo, -1\n" \
  "buffer_load_dwordx4 v234, s[16:19], s59 offen lds\n" \
  "s_mov_b32 exec_hi, -1\n" \
  "s_add_u32 s59, s59, 22016\n" \
  "v_exp_f32 v134, v134\n" \
  "v_exp_f32 v135, v135\n" \
  "v_exp_f32 v136, v136\n" \
  "v_exp_f32 v137, v137\n" \
  "s_waitcnt vmcnt(7)\n" \
  "v_smfmac_f32_32x32x32_bf16 v[64:79], v[192:195], v[128:135], v232\n" \
  "v_exp_f32 v138, v138\n" \
  "v_exp_f32 v139, v139\n" \
  "v_exp_f32 v140, v140\n" \
  "v_exp_f32 v141, v141\n" \
  "v_exp_f32 v142, v142\n" \
  "v_exp_f32 v143, v143\n" \
  "v_exp_f32 v144, v144\n" \
  "v_exp_f32 v145, v145\n" \
  "v_smfmac_f32_32x32x32_bf16 v[64:79], v[196:199], v[136:143], v232\n" \
  "v_exp_f32 v146, v146\n" \
  "v_exp_f32 v147, v147\n" \
  "v_exp_f32 v148, v148\n" \
  "v_mfma_f32_32x32x16_bf16 v[128:143], v[180:183], v[168:171], 0\n" \
  "v_exp_f32 v149, v149\n" \
  "v_exp_f32 v150, v150\n" \
  "v_exp_f32 v151, v151\n" \
  "v_exp_f32 v152, v152\n" \
  "v_exp_f32 v153, v153\n" \
  "v_smfmac_f32_32x32x32_bf16 v[80:95], v[192:195], v[144:151], v232\n" \
  "v_exp_f32 v154, v154\n" \
  "v_exp_f32 v155, v155\n" \
  "v_exp_f32 v156, v156\n" \
  "v_exp_f32 v157, v157\n" \
  "v_exp_f32 v158, v158\n" \
  "v_exp_f32 v159, v159\n" \
  "v_exp_f32 v128, v128\n" \
  "v_exp_f32 v129, v129\n" \
  "v_smfmac_f32_32x32x32_bf16 v[80:95], v[196:199], v[152:159], v232\n" \
  "v_exp_f32 v130, v130\n" \
  "v_exp_f32 v131, v131\n" \
  "v_exp_f32 v132, v132\n" \
  "v_mfma_f32_32x32x16_bf16 v[144:159], v[180:183], v[172:175], 0\n" \
  "v_exp_f32 v133, v133\n" \
  "buffer_load_dwordx4 v[180:183], v251, s[12:15], s56 offen offset:2048 sc1\n" \
  "buffer_load_dwordx4 v[36:39], v254, s[24:27], s72 offen\n" \
  "s_add_u32 s72, s72, 16384\n" \
  "v_exp_f32 v134, v134\n" \
  "v_exp_f32 v135, v135\n" \
  "v_exp_f32 v136, v136\n" \
  "v_exp_f32 v137, v137\n" \
  "v_smfmac_f32_32x32x32_bf16 v[96:111], v[192:195], v[128:135], v232\n" \
  "v_exp_f32 v138, v138\n" \
  "v_exp_f32 v139, v139\n" \
  "v_exp_f32 v140, v140\n" \
  "v_exp_f32 v141, v141\n" \
  "v_exp_f32 v142, v142\n" \
  "v_exp_f32 v143, v143\n" \
  "v_exp_f32 v144, v144\n" \
  "v_exp_f32 v145, v145\n" \
  "v_smfmac_f32_32x32x32_bf16 v[96:111], v[196:199], v[136:143], v232\n" \
  "v_exp_f32 v146, v146\n" \
  "v_exp_f32 v147, v147\n" \
  "v_exp_f32 v148, v148\n" \
  "s_waitcnt vmcnt(7)\n" \
  "v_mfma_f32_32x32x16_bf16 v[128:143], v[176:179], v[160:163], 0\n" \
  "v_exp_f32 v149, v149\n" \
  "v_exp_f32 v150, v150\n" \
  "v_exp_f32 v151, v151\n" \
  "v_exp_f32 v152, v152\n" \
  "v_exp_f32 v153, v153\n" \
  "v_smfmac_f32_32x32x32_bf16 v[112:127], v[192:195], v[144:151], v232\n" \
  "v_exp_f32 v154, v154\n" \
  "v_exp_f32 v155, v155\n" \
  "v_exp_f32 v156, v156\n" \
  "v_exp_f32 v157, v157\n" \
  "v_exp_f32 v158, v158\n" \
  "v_exp_f32 v159, v159\n" \
    \
  "v_exp_f32 v128, v128\n" \
  "v_exp_f32 v129, v129\n" \
  "v_smfmac_f32_32x32x32_bf16 v[112:127], v[196:199], v[152:159], v232\n" \
  "v_exp_f32 v130, v130\n" \
  "v_exp_f32 v131, v131\n" \
  "v_exp_f32 v132, v132\n" \
  "v_mfma_f32_32x32x16_bf16 v[144:159], v[176:179], v[164:167], 0\n" \
  "v_exp_f32 v133, v133\n" \
  "s_mov_b64 exec, s[76:77]\n" \
  "buffer_load_dwordx4 v[192:195], v233, s[12:15], s56 offen offset:2048 sc1\n" \
  "s_add_u32 m0, s58, 7680\n" \
  "buffer_load_dwordx4 v[196:199], v233, s[12:15], s56 offen offset:2560 sc1\n" \
  "s_mov_b32 exec_lo, -1\n" \
  "buffer_load_dwordx4 v234, s[16:19], s59 offen lds\n" \
  "s_mov_b32 exec_hi, -1\n" \
  "s_add_u32 s59, s59, 22016\n" \
  "v_exp_f32 v134, v134\n" \
  "v_exp_f32 v135, v135\n" \
  "v_exp_f32 v136, v136\n" \
  "v_exp_f32 v137, v137\n" \
  "s_waitcnt vmcnt(6)\n" \
  "v_smfmac_f32_32x32x32_bf16 v[64:79], v[184:187], v[128:135], v232\n" \
  "v_exp_f32 v138, v138\n" \
  "v_exp_f32 v139, v139\n" \
  "v_exp_f32 v140, v140\n" \
  "v_exp_f32 v141, v141\n" \
  "v_exp_f32 v142, v142\n" \
  "v_exp_f32 v143, v143\n" \
  "v_exp_f32 v144, v144\n" \
  "v_exp_f32 v145, v145\n" \
  "v_smfmac_f32_32x32x32_bf16 v[64:79], v[188:191], v[136:143], v232\n" \
  "v_exp_f32 v146, v146\n" \
  "v_exp_f32 v147, v147\n" \
  "v_exp_f32 v148, v148\n" \
  "v_mfma_f32_32x32x16_bf16 v[128:143], v[176:179], v[168:171], 0\n" \
  "v_exp_f32 v149, v149\n" \
  "v_exp_f32 v150, v150\n" \
  "v_exp_f32 v151, v151\n" \
  "v_exp_f32 v152, v152\n" \
  "v_exp_f32 v153, v153\n" \
  "v_smfmac_f32_32x32x32_bf16 v[80:95], v[184:187], v[144:151], v232\n" \
  "v_exp_f32 v154, v154\n" \
  "v_exp_f32 v155, v155\n" \
  "v_exp_f32 v156, v156\n" \
  "v_exp_f32 v157, v157\n" \
  "v_exp_f32 v158, v158\n" \
  "v_exp_f32 v159, v159\n" \
  "v_exp_f32 v128, v128\n" \
  "v_exp_f32 v129, v129\n" \
  "v_smfmac_f32_32x32x32_bf16 v[80:95], v[188:191], v[152:159], v232\n" \
  "v_exp_f32 v130, v130\n" \
  "v_exp_f32 v131, v131\n" \
  "v_exp_f32 v132, v132\n" \
  "v_mfma_f32_32x32x16_bf16 v[144:159], v[176:179], v[172:175], 0\n" \
  "v_exp_f32 v133, v133\n" \
  "s_xor_b32 s56, s56, 36864\n" \
  "buffer_load_dwordx4 v[176:179], v251, s[12:15], s56 offen offset:0 sc1\n" \
  "buffer_load_dwordx4 v[40:43], v254, s[24:27], s72 offen\n" \
  "s_add_u32 s72, s72, 16384\n" \
  "buffer_load_dwordx4 v[220:223], v254, s[20:23], s60 offen\n" \
  "s_xor_b32 s60, s60, 24576\n" \
  "v_exp_f32 v134, v134\n" \
  "v_exp_f32 v135, v135\n" \
  "v_exp_f32 v136, v136\n" \
  "v_exp_f32 v137, v137\n" \
  "v_smfmac_f32_32x32x32_bf16 v[96:111], v[184:187], v[128:135], v232\n" \
  "v_exp_f32 v138, v138\n" \
  "v_exp_f32 v139, v139\n" \
  "v_exp_f32 v140, v140\n" \
  "v_exp_f32 v141, v141\n" \
  "v_exp_f32 v142, v142\n" \
  "v_exp_f32 v143, v143\n" \
  "v_exp_f32 v144, v144\n" \
  "v_exp_f32 v145, v145\n" \
  "v_smfmac_f32_32x32x32_bf16 v[96:111], v[188:191], v[136:143], v232\n" \
  "v_exp_f32 v146, v146\n" \
  "v_exp_f32 v147, v147\n" \
  "v_exp_f32 v148, v148\n" \
  "s_waitcnt vmcnt(7)\n" \
  "v_mfma_f32_32x32x16_bf16 v[128:143], v[180:183], v[160:163], 0\n" \
  "v_exp_f32 v149, v149\n" \
  "v_exp_f32 v150, v150\n" \
  "v_exp_f32 v151, v151\n" \
  "v_exp_f32 v152, v152\n" \
  "v_exp_f32 v153, v153\n" \
  "v_smfmac_f32_32x32x32_bf16 v[112:127], v[184:187], v[144:151], v232\n" \
  "v_exp_f32 v154, v154\n" \
  "v_exp_f32 v155, v155\n" \
  "v_exp_f32 v156, v156\n" \
  "v_exp_f32 v157, v157\n" \
  "v_exp_f32 v158, v158\n" \
  "v_exp_f32 v159, v159\n" \
    \
  "v_exp_f32 v128, v128\n" \
  "v_exp_f32 v129, v129\n" \
  "v_smfmac_f32_32x32x32_bf16 v[112:127], v[188:191], v[152:159], v232\n" \
  "v_exp_f32 v130, v130\n" \
  "v_exp_f32 v131, v131\n" \
  "v_exp_f32 v132, v132\n" \
  "v_mfma_f32_32x32x16_bf16 v[144:159], v[180:183], v[164:167], 0\n" \
  "v_exp_f32 v133, v133\n" \
  "s_mov_b64 exec, s[76:77]\n" \
  "buffer_load_dwordx4 v[184:187], v233, s[12:15], s56 offen offset:0 sc1\n" \
  "s_add_u32 m0, s58, 8448\n" \
  "buffer_load_dwordx4 v[188:191], v233, s[12:15], s56 offen offset:512 sc1\n" \
  "s_mov_b32 exec_lo, -1\n" \
  "buffer_load_dwordx4 v234, s[16:19], s59 offen lds\n" \
  "s_mov_b32 exec_hi, -1\n" \
  "s_add_u32 s59, s59, 22016\n" \
  "v_exp_f32 v134, v134\n" \
  "v_exp_f32 v135, v135\n" \
  "v_exp_f32 v136, v136\n" \
  "v_exp_f32 v137, v137\n" \
  "s_waitcnt vmcnt(7)\n" \
  "v_smfmac_f32_32x32x32_bf16 v[64:79], v[192:195], v[128:135], v232\n" \
  "v_exp_f32 v138, v138\n" \
  "v_exp_f32 v139, v139\n" \
  "v_exp_f32 v140, v140\n" \
  "v_exp_f32 v141, v141\n" \
  "v_exp_f32 v142, v142\n" \
  "v_exp_f32 v143, v143\n" \
  "v_exp_f32 v144, v144\n" \
  "v_exp_f32 v145, v145\n" \
  "v_smfmac_f32_32x32x32_bf16 v[64:79], v[196:199], v[136:143], v232\n" \
  "v_exp_f32 v146, v146\n" \
  "v_exp_f32 v147, v147\n" \
  "v_exp_f32 v148, v148\n" \
  "v_mfma_f32_32x32x16_bf16 v[128:143], v[180:183], v[168:171], 0\n" \
  "v_exp_f32 v149, v149\n" \
  "v_exp_f32 v150, v150\n" \
  "v_exp_f32 v151, v151\n" \
  "v_exp_f32 v152, v152\n" \
  "v_exp_f32 v153, v153\n" \
  "v_smfmac_f32_32x32x32_bf16 v[80:95], v[192:195], v[144:151], v232\n" \
  "v_exp_f32 v154, v154\n" \
  "v_exp_f32 v155, v155\n" \
  "v_exp_f32 v156, v156\n" \
  "v_exp_f32 v157, v157\n" \
  "v_exp_f32 v158, v158\n" \
  "v_exp_f32 v159, v159\n" \
  "v_exp_f32 v128, v128\n" \
  "v_exp_f32 v129, v129\n" \
  "v_smfmac_f32_32x32x32_bf16 v[80:95], v[196:199], v[152:159], v232\n" \
  "v_exp_f32 v130, v130\n" \
  "v_exp_f32 v131, v131\n" \
  "v_exp_f32 v132, v132\n" \
  "v_mfma_f32_32x32x16_bf16 v[144:159], v[180:183], v[172:175], 0\n" \
  "v_exp_f32 v133, v133\n" \
  "buffer_load_dwordx4 v[180:183], v251, s[12:15], s56 offen offset:2048 sc1\n" \
  "buffer_load_dwordx4 v[44:47], v254, s[24:27], s72 offen\n" \
  "s_add_u32 s72, s72, 65536\n" \
  "v_exp_f32 v134, v134\n" \
  "v_exp_f32 v135, v135\n" \
  "v_exp_f32 v136, v136\n" \
  "v_exp_f32 v137, v137\n" \
  "v_smfmac_f32_32x32x32_bf16 v[96:111], v[192:195], v[128:135], v232\n" \
  "v_exp_f32 v138, v138\n" \
  "v_exp_f32 v139, v139\n" \
  "v_exp_f32 v140, v140\n" \
  "v_exp_f32 v141, v141\n" \
  "v_exp_f32 v142, v142\n" \
  "v_exp_f32 v143, v143\n" \
  "v_exp_f32 v144, v144\n" \
  "v_exp_f32 v145, v145\n" \
  "v_smfmac_f32_32x32x32_bf16 v[96:111], v[196:199], v[136:143], v232\n" \
  "v_exp_f32 v146, v146\n" \
  "v_exp_f32 v147, v147\n" \
  "v_exp_f32 v148, v148\n" \
  "s_waitcnt vmcnt(7)\n" \
  "v_mfma_f32_32x32x16_bf16 v[128:143], v[176:179], v[160:163], 0\n" \
  "v_exp_f32 v149, v149\n" \
  "v_exp_f32 v150, v150\n" \
  "v_exp_f32 v151, v151\n" \
  "v_exp_f32 v152, v152\n" \
  "v_exp_f32 v153, v153\n" \
  "v_smfmac_f32_32x32x32_bf16 v[112:127], v[192:195], v[144:151], v232\n" \
  "v_exp_f32 v154, v154\n" \
  "v_exp_f32 v155, v155\n" \
  "v_exp_f32 v156, v156\n" \
  "v_exp_f32 v157, v157\n" \
  "v_exp_f32 v158, v158\n" \
  "v_exp_f32 v159, v159\n" \
    \
  "v_exp_f32 v128, v128\n" \
  "v_exp_f32 v129, v129\n" \
  "v_smfmac_f32_32x32x32_bf16 v[112:127], v[196:199], v[152:159], v232\n" \
  "v_exp_f32 v130, v130\n" \
  "v_exp_f32 v131, v131\n" \
  "v_exp_f32 v132, v132\n" \
  "v_mfma_f32_32x32x16_bf16 v[144:159], v[176:179], v[164:167], 0\n" \
  "v_exp_f32 v133, v133\n" \
  "s_mov_b64 exec, s[76:77]\n" \
  "buffer_load_dwordx4 v[192:195], v233, s[12:15], s56 offen offset:2048 sc1\n" \
  "s_add_u32 m0, s58, 9216\n" \
  "buffer_load_dwordx4 v[196:199], v233, s[12:15], s56 offen offset:2560 sc1\n" \
  "s_mov_b32 exec_lo, -1\n" \
  "buffer_load_dwordx4 v234, s[16:19], s59 offen lds\n" \
  "s_mov_b32 exec_hi, -1\n" \
  "s_add_u32 s59, s59, 22016\n" \
  "v_exp_f32 v134, v134\n" \
  "v_exp_f32 v135, v135\n" \
  "v_exp_f32 v136, v136\n" \
  "v_exp_f32 v137, v137\n" \
  "s_waitcnt vmcnt(6)\n" \
  "v_smfmac_f32_32x32x32_bf16 v[64:79], v[184:187], v[128:135], v232\n" \
  "v_exp_f32 v138, v138\n" \
  "v_exp_f32 v139, v139\n" \
  "v_exp_f32 v140, v140\n" \
  "v_exp_f32 v141, v141\n" \
  "v_exp_f32 v142, v142\n" \
  "v_exp_f32 v143, v143\n" \
  "v_exp_f32 v144, v144\n" \
  "v_exp_f32 v145, v145\n" \
  "v_smfmac_f32_32x32x32_bf16 v[64:79], v[188:191], v[136:143], v232\n" \
  "v_exp_f32 v146, v146\n" \
  "v_exp_f32 v147, v147\n" \
  "v_exp_f32 v148, v148\n" \
  "v_mfma_f32_32x32x16_bf16 v[128:143], v[176:179], v[168:171], 0\n" \
  "v_exp_f32 v149, v149\n" \
  "v_exp_f32 v150, v150\n" \
  "v_exp_f32 v151, v151\n" \
  "v_exp_f32 v152, v152\n" \
  "v_exp_f32 v153, v153\n" \
  "v_smfmac_f32_32x32x32_bf16 v[80:95], v[184:187], v[144:151], v232\n" \
  "v_exp_f32 v154, v154\n" \
  "v_exp_f32 v155, v155\n" \
  "v_exp_f32 v156, v156\n" \
  "v_exp_f32 v157, v157\n" \
  "v_exp_f32 v158, v158\n" \
  "v_exp_f32 v159, v159\n" \
  "v_exp_f32 v128, v128\n" \
  "v_exp_f32 v129, v129\n" \
  "v_smfmac_f32_32x32x32_bf16 v[80:95], v[188:191], v[152:159], v232\n" \
  "v_exp_f32 v130, v130\n" \
  "v_exp_f32 v131, v131\n" \
  "v_exp_f32 v132, v132\n" \
  "v_mfma_f32_32x32x16_bf16 v[144:159], v[176:179], v[172:175], 0\n" \
  "v_exp_f32 v133, v133\n" \
  "s_xor_b32 s56, s56, 4096\n" \
  "buffer_load_dwordx4 v[176:179], v251, s[12:15], s56 offen offset:0 sc1\n" \
  "buffer_load_dwordx4 v[48:51], v254, s[24:27], s72 offen\n" \
  "s_add_u32 s72, s72, 16384\n" \
  "buffer_load_dwordx4 v[224:227], v254, s[20:23], s60 offen\n" \
  "s_xor_b32 s60, s60, 8192\n" \
  "v_exp_f32 v134, v134\n" \
  "v_exp_f32 v135, v135\n" \
  "v_exp_f32 v136, v136\n" \
  "v_exp_f32 v137, v137\n" \
  "v_smfmac_f32_32x32x32_bf16 v[96:111], v[184:187], v[128:135], v232\n" \
  "v_exp_f32 v138, v138\n" \
  "v_exp_f32 v139, v139\n" \
  "v_exp_f32 v140, v140\n" \
  "v_exp_f32 v141, v141\n" \
  "v_exp_f32 v142, v142\n" \
  "v_exp_f32 v143, v143\n" \
  "v_exp_f32 v144, v144\n" \
  "v_exp_f32 v145, v145\n" \
  "v_smfmac_f32_32x32x32_bf16 v[96:111], v[188:191], v[136:143], v232\n" \
  "v_exp_f32 v146, v146\n" \
  "v_exp_f32 v147, v147\n" \
  "v_exp_f32 v148, v148\n" \
  "s_waitcnt vmcnt(7)\n" \
  "v_mfma_f32_32x32x16_bf16 v[128:143], v[180:183], v[160:163], 0\n" \
  "v_exp_f32 v149, v149\n" \
  "v_exp_f32 v150, v150\n" \
  "v_exp_f32 v151, v151\n" \
  "v_exp_f32 v152, v152\n" \
  "v_exp_f32 v153, v153\n" \
  "v_smfmac_f32_32x32x32_bf16 v[112:127], v[184:187], v[144:151], v232\n" \
  "v_exp_f32 v154, v154\n" \
  "v_exp_f32 v155, v155\n" \
  "v_exp_f32 v156, v156\n" \
  "v_exp_f32 v157, v157\n" \
  "v_exp_f32 v158, v158\n" \
  "v_exp_f32 v159, v159\n" \
    \
  "v_exp_f32 v128, v128\n" \
  "v_exp_f32 v129, v129\n" \
  "v_smfmac_f32_32x32x32_bf16 v[112:127], v[188:191], v[152:159], v232\n" \
  "v_exp_f32 v130, v130\n" \
  "v_exp_f32 v131, v131\n" \
  "v_exp_f32 v132, v132\n" \
  "v_mfma_f32_32x32x16_bf16 v[144:159], v[180:183], v[164:167], 0\n" \
  "v_exp_f32 v133, v133\n" \
  "s_mov_b64 exec, s[76:77]\n" \
  "buffer_load_dwordx4 v[184:187], v233, s[12:15], s56 offen offset:0 sc1\n" \
  "s_add_u32 m0, s58, 9984\n" \
  "buffer_load_dwordx4 v[188:191], v233, s[12:15], s56 offen offset:512 sc1\n" \
  "s_mov_b32 exec_lo, -1\n" \
  "buffer_load_dwordx4 v234, s[16:19], s59 offen lds\n" \
  "s_mov_b32 exec_hi, -1\n" \
  "s_add_u32 s59, s59, 22016\n" \
  "v_exp_f32 v134, v134\n" \
  "v_exp_f32 v135, v135\n" \
  "v_exp_f32 v136, v136\n" \
  "v_exp_f32 v137, v137\n" \
  "s_waitcnt vmcnt(7)\n" \
  "v_smfmac_f32_32x32x32_bf16 v[64:79], v[192:195], v[128:135], v232\n" \
  "v_exp_f32 v138, v138\n" \
  "v_exp_f32 v139, v139\n" \
  "v_exp_f32 v140, v140\n" \
  "v_exp_f32 v141, v141\n" \
  "v_exp_f32 v142, v142\n" \
  "v_exp_f32 v143, v143\n" \
  "v_exp_f32 v144, v144\n" \
  "v_exp_f32 v145, v145\n" \
  "v_smfmac_f32_32x32x32_bf16 v[64:79], v[196:199], v[136:143], v232\n" \
  "v_exp_f32 v146, v146\n" \
  "v_exp_f32 v147, v147\n" \
  "v_exp_f32 v148, v148\n" \
  "v_mfma_f32_32x32x16_bf16 v[128:143], v[180:183], v[168:171], 0\n" \
  "v_exp_f32 v149, v149\n" \
    \
  "s_cmp_lg_u32 s36, 7\n" \
  "s_cbranch_scc1 fbk_b5\n" \
  "s_mov_b32 exec_lo, 0\n" \
  "s_mov_b32 exec_hi, 0xff\n" \
  "v_mov_b32 v249, 0\n" \
  "buffer_atomic_add v249, v252, s[12:15], 0 offen sc0\n" \
  "s_mov_b64 exec, -1\n" \
  "fbk_b5:\n" \
  "v_exp_f32 v150, v150\n" \
  "v_exp_f32 v151, v151\n" \
  "v_exp_f32 v152, v152\n" \
  "v_exp_f32 v153, v153\n" \
  "v_smfmac_f32_32x32x32_bf16 v[80:95], v[192:195], v[144:151], v232\n" \
  "v_exp_f32 v154, v154\n" \
  "v_exp_f32 v155, v155\n" \
  "v_exp_f32 v156, v156\n" \
  "v_exp_f32 v157, v157\n" \
  "v_exp_f32 v158, v158\n" \
  "v_exp_f32 v159, v159\n" \
  "s_setprio 1\n" \
  "v_exp_f32 v128, v128\n" \
  "v_exp_f32 v129, v129\n" \
  "v_smfmac_f32_32x32x32_bf16 v[80:95], v[196:199], v[152:159], v232\n" \
  "v_exp_f32 v130, v130\n" \
  "v_exp_f32 v131, v131\n" \
  "v_exp_f32 v132, v132\n" \
  "v_mfma_f32_32x32x16_bf16 v[144:159], v[180:183], v[172:175], 0\n" \
  "v_exp_f32 v133, v133\n" \
  "buffer_load_dwordx4 v[180:183], v251, s[12:15], s56 offen offset:2048 sc1\n" \
  "buffer_load_dwordx4 v[52:55], v254, s[24:27], s72 offen\n" \
  "s_add_u32 s72, s72, 16384\n" \
    \
  "s_add_u32 s57, s57, fbk_b2-fbk_code_begin\n" \
  "buffer_load_dword v244, off, s[32:35], s57\n" \
  "v_exp_f32 v134, v134\n" \
  "v_exp_f32 v135, v135\n" \
  "v_exp_f32 v136, v136\n" \
  "v_exp_f32 v137, v137\n" \
  "v_smfmac_f32_32x32x32_bf16 v[96:111], v[192:195], v[128:135], v232\n" \
  "v_exp_f32 v138, v138\n" \
  "v_exp_f32 v139, v139\n" \
  "v_exp_f32 v140, v140\n" \
  "v_exp_f32 v141, v141\n" \
  "v_exp_f32 v142, v142\n" \
  "v_exp_f32 v143, v143\n" \
  "v_exp_f32 v144, v144\n" \
  "v_exp_f32 v145, v145\n" \
  "v_smfmac_f32_32x32x32_bf16 v[96:111], v[196:199], v[136:143], v232\n" \
  "v_exp_f32 v146, v146\n" \
  "v_exp_f32 v147, v147\n" \
  "v_exp_f32 v148, v148\n" \
  "s_waitcnt vmcnt(8)\n" \
  "v_mfma_f32_32x32x16_bf16 v[128:143], v[176:179], v[160:163], 0\n" \
  "v_exp_f32 v149, v149\n" \
  "v_exp_f32 v150, v150\n" \
  "v_exp_f32 v151, v151\n" \
  "v_exp_f32 v152, v152\n" \
  "v_exp_f32 v153, v153\n" \
  "v_smfmac_f32_32x32x32_bf16 v[112:127], v[192:195], v[144:151], v232\n" \
  "v_exp_f32 v154, v154\n" \
  "v_exp_f32 v155, v155\n" \
  "v_exp_f32 v156, v156\n" \
  "v_exp_f32 v157, v157\n" \
  "v_exp_f32 v158, v158\n" \
  "v_exp_f32 v159, v159\n" \
    \
  "v_exp_f32 v128, v128\n" \
  "v_exp_f32 v129, v129\n" \
  "v_smfmac_f32_32x32x32_bf16 v[112:127], v[196:199], v[152:159], v232\n" \
  "v_exp_f32 v130, v130\n" \
  "v_exp_f32 v131, v131\n" \
  "v_exp_f32 v132, v132\n" \
  "v_mfma_f32_32x32x16_bf16 v[144:159], v[176:179], v[164:167], 0\n" \
  "v_exp_f32 v133, v133\n" \
  "s_mov_b64 exec, s[76:77]\n" \
  "buffer_load_dwordx4 v[192:195], v233, s[12:15], s56 offen offset:2048 sc1\n" \
  "s_add_u32 m0, s58, 10752\n" \
  "buffer_load_dwordx4 v[196:199], v233, s[12:15], s56 offen offset:2560 sc1\n" \
  "s_mov_b32 exec_lo, -1\n" \
  "buffer_load_dwordx4 v234, s[16:19], s59 offen lds\n" \
  "s_mov_b32 exec_hi, -1\n" \
  "s_add_u32 s59, s59, 22016\n" \
  "v_exp_f32 v134, v134\n" \
  "v_exp_f32 v135, v135\n" \
  "v_exp_f32 v136, v136\n" \
  "v_exp_f32 v137, v137\n" \
  "s_waitcnt vmcnt(7)\n" \
  "v_smfmac_f32_32x32x32_bf16 v[64:79], v[184:187], v[128:135], v232\n" \
  "v_exp_f32 v138, v138\n" \
  "v_exp_f32 v139, v139\n" \
  "v_exp_f32 v140, v140\n" \
  "v_exp_f32 v141, v141\n" \
  "v_exp_f32 v142, v142\n" \
  "v_exp_f32 v143, v143\n" \
  "v_exp_f32 v144, v144\n" \
  "v_exp_f32 v145, v145\n" \
  "v_smfmac_f32_32x32x32_bf16 v[64:79], v[188:191], v[136:143], v232\n" \
  "v_exp_f32 v146, v146\n" \
  "v_exp_f32 v147, v147\n" \
  "v_exp_f32 v148, v148\n" \
  "v_mfma_f32_32x32x16_bf16 v[128:143], v[176:179], v[168:171], 0\n" \
  "v_exp_f32 v149, v149\n" \
  "v_exp_f32 v150, v150\n" \
  "v_exp_f32 v151, v151\n" \
  "v_exp_f32 v152, v152\n" \
  "v_exp_f32 v153, v153\n" \
  "v_smfmac_f32_32x32x32_bf16 v[80:95], v[184:187], v[144:151], v232\n" \
  "v_exp_f32 v154, v154\n" \
  "v_exp_f32 v155, v155\n" \
  "v_exp_f32 v156, v156\n" \
  "v_exp_f32 v157, v157\n" \
  "v_exp_f32 v158, v158\n" \
  "v_exp_f32 v159, v159\n" \
  "v_exp_f32 v128, v128\n" \
  "v_exp_f32 v129, v129\n" \
  "v_smfmac_f32_32x32x32_bf16 v[80:95], v[188:191], v[152:159], v232\n" \
  "v_exp_f32 v130, v130\n" \
  "v_exp_f32 v131, v131\n" \
  "v_exp_f32 v132, v132\n" \
  "v_mfma_f32_32x32x16_bf16 v[144:159], v[176:179], v[172:175], 0\n" \
  "v_exp_f32 v133, v133\n" \
    \
  "v_lshlrev_b32 v246, 10, v250\n" \
  "v_lshl_or_b32 v246, v250, 1, v246\n" \
  "v_and_b32 v246, 0x1c70, v246\n" \
  "buffer_load_dwordx4 v[176:179], v246, s[4:7], s82 offen\n" \
  "buffer_load_dwordx4 v[56:59], v254, s[24:27], s72 offen\n" \
  "buffer_load_dwordx4 v[228:231], v254, s[20:23], s60 offen\n" \
  "v_exp_f32 v134, v134\n" \
  "v_exp_f32 v135, v135\n" \
  "v_exp_f32 v136, v136\n" \
  "v_exp_f32 v137, v137\n" \
  "v_smfmac_f32_32x32x32_bf16 v[96:111], v[184:187], v[128:135], v232\n" \
  "v_exp_f32 v138, v138\n" \
  "v_exp_f32 v139, v139\n" \
  "v_exp_f32 v140, v140\n" \
  "v_exp_f32 v141, v141\n" \
  "v_exp_f32 v142, v142\n" \
  "v_exp_f32 v143, v143\n" \
  "v_exp_f32 v144, v144\n" \
  "v_exp_f32 v145, v145\n" \
  "v_smfmac_f32_32x32x32_bf16 v[96:111], v[188:191], v[136:143], v232\n" \
  "v_exp_f32 v146, v146\n" \
  "v_exp_f32 v147, v147\n" \
  "v_exp_f32 v148, v148\n" \
  "s_waitcnt vmcnt(8)\n" \
  "v_mfma_f32_32x32x16_bf16 v[128:143], v[180:183], v[160:163], 0\n" \
  "v_exp_f32 v149, v149\n" \
  "v_exp_f32 v150, v150\n" \
  "v_exp_f32 v151, v151\n" \
  "v_exp_f32 v152, v152\n" \
  "v_exp_f32 v153, v153\n" \
  "v_smfmac_f32_32x32x32_bf16 v[112:127], v[184:187], v[144:151], v232\n" \
  "v_exp_f32 v154, v154\n" \
  "v_exp_f32 v155, v155\n" \
  "v_exp_f32 v156, v156\n" \
  "v_exp_f32 v157, v157\n" \
  "v_exp_f32 v158, v158\n" \
  "v_exp_f32 v159, v159\n" \
    \
  "v_exp_f32 v128, v128\n" \
  "v_exp_f32 v129, v129\n" \
  "v_smfmac_f32_32x32x32_bf16 v[112:127], v[188:191], v[152:159], v232\n" \
  "v_exp_f32 v130, v130\n" \
  "v_exp_f32 v131, v131\n" \
  "v_exp_f32 v132, v132\n" \
  "v_mfma_f32_32x32x16_bf16 v[144:159], v[180:183], v[164:167], 0\n" \
  "v_exp_f32 v133, v133\n" \
  "s_add_u32 m0, s58, 11520\n" \
  "s_mov_b32 exec_hi, 0xffff\n" \
  "buffer_load_dwordx4 v234, s[16:19], s59 offen lds\n" \
  "s_mov_b32 exec_hi, -1\n" \
  "v_exp_f32 v134, v134\n" \
  "v_exp_f32 v135, v135\n" \
  "v_exp_f32 v136, v136\n" \
  "v_exp_f32 v137, v137\n" \
  "s_waitcnt vmcnt(5)\n" \
  "v_smfmac_f32_32x32x32_bf16 v[64:79], v[192:195], v[128:135], v232\n" \
  "v_exp_f32 v138, v138\n" \
  "v_exp_f32 v139, v139\n" \
  "v_exp_f32 v140, v140\n" \
  "v_exp_f32 v141, v141\n" \
  "v_exp_f32 v142, v142\n" \
  "v_exp_f32 v143, v143\n" \
  "v_exp_f32 v144, v144\n" \
  "v_exp_f32 v145, v145\n" \
  "v_smfmac_f32_32x32x32_bf16 v[64:79], v[196:199], v[136:143], v232\n" \
  "v_exp_f32 v146, v146\n" \
  "v_exp_f32 v147, v147\n" \
  "v_exp_f32 v148, v148\n" \
  "v_mfma_f32_32x32x16_bf16 v[128:143], v[180:183], v[168:171], 0\n" \
  "v_exp_f32 v149, v149\n" \
  "v_exp_f32 v150, v150\n" \
  "v_exp_f32 v151, v151\n" \
  "v_exp_f32 v152, v152\n" \
  "v_exp_f32 v153, v153\n" \
  "v_smfmac_f32_32x32x32_bf16 v[80:95], v[192:195], v[144:151], v232\n" \
  "v_exp_f32 v154, v154\n" \
  "v_exp_f32 v155, v155\n" \
  "v_exp_f32 v156, v156\n" \
  "v_exp_f32 v157, v157\n" \
  "v_exp_f32 v158, v158\n" \
    \
  "ds_write_b128 v236, v[64:67]\n" \
  "ds_write_b128 v236, v[68:71] offset:1024\n" \
  "ds_write_b32 v239, v72\n" \
  "v_exp_f32 v159, v159\n" \
  "s_setprio 0\n" \
  "v_exp_f32 v128, v128\n" \
  "v_exp_f32 v129, v129\n" \
  "v_smfmac_f32_32x32x32_bf16 v[80:95], v[196:199], v[152:159], v232\n" \
  "v_exp_f32 v130, v130\n" \
  "v_exp_f32 v131, v131\n" \
  "v_exp_f32 v132, v132\n" \
  "v_mfma_f32_32x32x16_bf16 v[144:159], v[180:183], v[172:175], 0\n" \
  "v_exp_f32 v133, v133\n" \
  "buffer_load_dwordx4 v[60:63], v254, s[24:27], s73 offen\n" \
  "v_exp_f32 v134, v134\n" \
  "v_exp_f32 v135, v135\n" \
  "v_exp_f32 v136, v136\n" \
  "v_exp_f32 v137, v137\n" \
  "v_smfmac_f32_32x32x32_bf16 v[96:111], v[192:195], v[128:135], v232\n" \
  "v_exp_f32 v138, v138\n" \
  "v_exp_f32 v139, v139\n" \
  "v_exp_f32 v140, v140\n" \
  "v_exp_f32 v141, v141\n" \
  "v_exp_f32 v142, v142\n" \
  "ds_write_b128 v237, v[80:83]\n" \
  "ds_write_b128 v237, v[84:87] offset:1024\n" \
  "ds_write_b32 v240, v88\n" \
  "v_exp_f32 v143, v143\n" \
  "v_exp_f32 v144, v144\n" \
  "v_exp_f32 v145, v145\n" \
  "v_smfmac_f32_32x32x32_bf16 v[96:111], v[196:199], v[136:143], v232\n" \
  "v_exp_f32 v146, v146\n" \
  "v_exp_f32 v147, v147\n" \
  "v_exp_f32 v148, v148\n" \
  "v_exp_f32 v149, v149\n" \
  "v_exp_f32 v150, v150\n" \
  "v_exp_f32 v151, v151\n" \
  "v_exp_f32 v152, v152\n" \
  "v_exp_f32 v153, v153\n" \
  "v_smfmac_f32_32x32x32_bf16 v[112:127], v[192:195], v[144:151], v232\n" \
  "v_exp_f32 v154, v154\n" \
  "v_exp_f32 v155, v155\n" \
  "v_exp_f32 v156, v156\n" \
  "v_exp_f32 v157, v157\n" \
  "v_exp_f32 v158, v158\n" \
  "ds_write_b128 v238, v[96:99]\n" \
  "ds_write_b128 v238, v[100:103] offset:1024\n" \
  "ds_write_b32 v241, v104\n" \
  "v_exp_f32 v159, v159\n" \
  "s_nop 1\n" \
  "v_smfmac_f32_32x32x32_bf16 v[112:127], v[196:199], v[152:159], v232\n" \
    \
  "s_waitcnt lgkmcnt(0)\n" \
  "s_barrier\n" \
  "ds_read_b32 v88, v243 offset:0\n" \
  "ds_read_b32 v89, v243 offset:256\n" \
  "ds_read_b32 v90, v243 offset:512\n" \
  "ds_read_b128 v[64:67], v242 offset:0\n" \
  "ds_read_b128 v[68:71], v242 offset:1024\n" \
  "ds_read_b128 v[72:75], v242 offset:2048\n" \
  "ds_read_b128 v[76:79], v242 offset:3072\n" \
  "ds_read_b128 v[80:83], v242 offset:4096\n" \
  "ds_read_b128 v[84:87], v242 offset:5120\n" \
  "s_waitcnt lgkmcnt(7)\n" \
  "v_add_f32 v88, v88, v89\n" \
  "s_waitcnt lgkmcnt(6)\n" \
  "v_add_f32 v120, v120, v90\n" \
  "v_add_f32 v120, v120, v88\n" \
    \
  "v_mov_b32 v121, v120\n" \
  "s_waitcnt lgkmcnt(5)\n" \
  "v_pk_add_f32 v[112:113], v[112:113], v[64:65]\n" \
  "v_pk_add_f32 v[114:115], v[114:115], v[66:67]\n" \
  "s_waitcnt lgkmcnt(4)\n" \
  "v_pk_add_f32 v[116:117], v[116:117], v[68:69]\n" \
  "v_pk_add_f32 v[118:119], v[118:119], v[70:71]\n" \
  "v_permlane32_swap_b32 v120, v121\n" \
  "v_add_f32 v120, v120, v121\n" \
  "s_waitcnt lgkmcnt(3)\n" \
  "v_pk_add_f32 v[112:113], v[112:113], v[72:73]\n" \
  "v_pk_add_f32 v[114:115], v[114:115], v[74:75]\n" \
  "s_waitcnt lgkmcnt(2)\n" \
  "v_pk_add_f32 v[116:117], v[116:117], v[76:77]\n" \
  "v_pk_add_f32 v[118:119], v[118:119], v[78:79]\n" \
  "v_rcp_f32 v122, v120\n" \
  "s_waitcnt lgkmcnt(1)\n" \
  "v_pk_add_f32 v[112:113], v[112:113], v[80:81]\n" \
  "v_pk_add_f32 v[114:115], v[114:115], v[82:83]\n" \
  "s_waitcnt lgkmcnt(0)\n" \
  "v_pk_add_f32 v[116:117], v[116:117], v[84:85]\n" \
  "v_pk_add_f32 v[118:119], v[118:119], v[86:87]\n" \
  "v_pk_mul_f32 v[112:113], v[112:113], v[122:123] op_sel_hi:[1,0]\n" \
  "v_pk_mul_f32 v[114:115], v[114:115], v[122:123] op_sel_hi:[1,0]\n" \
  "v_pk_mul_f32 v[116:117], v[116:117], v[122:123] op_sel_hi:[1,0]\n" \
  "v_pk_mul_f32 v[118:119], v[118:119], v[122:123] op_sel_hi:[1,0]\n" \
  "v_cvt_pk_bf16_f32 v124, v112, v113\n" \
  "v_cvt_pk_bf16_f32 v125, v114, v115\n" \
  "v_cvt_pk_bf16_f32 v126, v116, v117\n" \
  "v_cvt_pk_bf16_f32 v127, v118, v119\n" \
  "buffer_store_dwordx4 v[124:127], v251, s[12:15], s61 offen sc1\n" \
    \
    \
    \
  "v_cmp_nle_f32 vcc, 0x1f800000, v120\n" \
  "s_mov_b64 s[78:79], vcc\n" \
  "v_cmp_nge_f32 vcc, 0x71800000, v120\n" \
  "s_or_b64 s[78:79], s[78:79], vcc\n" \
  "v_add_f32 v64, |v112|, |v113|\n" \
  "v_add_f32 v64, v64, |v114|\n" \
  "v_add_f32 v64, v64, |v115|\n" \
  "v_add_f32 v64, v64, |v116|\n" \
  "v_add_f32 v64, v64, |v117|\n" \
  "v_add_f32 v64, v64, |v118|\n" \
  "v_add_f32 v64, v64, |v119|\n" \
  "v_cmp_nge_f32 vcc, 0x7f7fffff, v64\n" \
  "s_or_b64 vcc, s[78:79], vcc\n" \
  "s_cbranch_vccnz fbk_fallback\n" \
  "fbk_b2:\n" \
    \
  "v_and_b32 v140, 7, v250\n" \
  "v_lshrrev_b32 v141, 3, v250\n" \
  "v_lshrrev_b32 v142, 5, v250\n" \
  "v_bfe_u32 v143, v250, 2, 2\n" \
  "v_bfe_u32 v144, v250, 4, 1\n" \
  "s_lshl_b32 s47, s53, 2\n" \
  "s_add_u32 s47, s47, 98304\n" \
  "v_add_u32 v64, s47, v251\n" \
  "s_lshl2_add_u32 s50, s37, s40\n" \
  "v_and_b32 v145, 3, v250\n" \
  "v_lshlrev_b32 v145, 3, v145\n" \
  "v_lshl_or_b32 v146, v142, 1, v144\n" \
  "v_lshl_or_b32 v146, v146, 2, v143\n" \
  "v_mul_u32_u24 v146, 96, v146\n" \
  "v_mul_u32_u24 v147, 49152, v144\n" \
  "v_add3_u32 v246, v146, v147, v145\n" \
  "v_mul_u32_u24 v147, 768, v144\n" \
  "v_sub_u32 v247, v246, v147\n" \
  "v_add_u32 v247, 384, v247\n" \
  "s_mul_i32 s47, s45, 12288\n" \
  "v_add_u32 v72, s47, v246\n" \
  "v_add_u32 v73, s47, v247\n" \
  "s_lshr_b32 s47, s42, 1\n" \
  "s_xor_b32 s57, s47, s37\n" \
  "s_cmp_ge_u32 s36, 4\n" \
  "s_cbranch_scc1 fbk_c1\n" \
    \
  "v_mov_b32 v76, 0\n" \
  "v_mov_b32 v77, 0\n" \
  "v_mov_b32 v78, 0\n" \
  "ds_read_b64_tr_b16 v[84:85], v72 offset:0\n" \
  "ds_read_b64_tr_b16 v[86:87], v73 offset:0\n" \
  "ds_read_b64_tr_b16 v[88:89], v72 offset:32\n" \
  "ds_read_b64_tr_b16 v[90:91], v73 offset:32\n" \
  "ds_read_b64_tr_b16 v[92:93], v72 offset:64\n" \
  "ds_read_b64_tr_b16 v[94:95], v73 offset:64\n" \
  "ds_read_b64_tr_b16 v[96:97], v72 offset:1536\n" \
  "ds_read_b64_tr_b16 v[98:99], v73 offset:1536\n" \
  "ds_read_b64_tr_b16 v[100:101], v72 offset:1568\n" \
  "ds_read_b64_tr_b16 v[102:103], v73 offset:1568\n" \
  "ds_read_b64_tr_b16 v[104:105], v72 offset:1600\n" \
  "ds_read_b64_tr_b16 v[106:107], v73 offset:1600\n" \
  "ds_read_b64_tr_b16 v[108:109], v72 offset:3072\n" \
  "ds_read_b64_tr_b16 v[110:111], v73 offset:3072\n" \
  "ds_read_b64_tr_b16 v[112:113], v72 offset:3104\n" \
  "ds_read_b64_tr_b16 v[114:115], v73 offset:3104\n" \
  "ds_read_b64_tr_b16 v[116:117], v72 offset:3136\n" \
  "ds_read_b64_tr_b16 v[118:119], v73 offset:3136\n" \
  "s_waitcnt lgkmcnt(12)\n" \
  "v_dot2c_f32_bf16 v76, 0x3f803f80, v84\n" \
  "v_dot2c_f32_bf16 v77, 0x3f803f80, v88\n" \
  "v_dot2c_f32_bf16 v78, 0x3f803f80, v92\n" \
  "v_dot2c_f32_bf16 v76, 0x3f803f80, v85\n" \
  "v_dot2c_f32_bf16 v77, 0x3f803f80, v89\n" \
  "v_dot2c_f32_bf16 v78, 0x3f803f80, v93\n" \
  "v_dot2c_f32_bf16 v76, 0x3f803f80, v86\n" \
  "v_dot2c_f32_bf16 v77, 0x3f803f80, v90\n" \
  "v_dot2c_f32_bf16 v78, 0x3f803f80, v94\n" \
  "v_dot2c_f32_bf16 v76, 0x3f803f80, v87\n" \
  "v_dot2c_f32_bf16 v77, 0x3f803f80, v91\n" \
  "v_dot2c_f32_bf16 v78, 0x3f803f80, v95\n" \
  "ds_read_b64_tr_b16 v[84:85], v72 offset:4608\n" \
  "ds_read_b64_tr_b16 v[86:87], v73 offset:4608\n" \
  "ds_read_b64_tr_b16 v[88:89], v72 offset:4640\n" \
  "ds_read_b64_tr_b16 v[90:91], v73 offset:4640\n" \
  "ds_read_b64_tr_b16 v[92:93], v72 offset:4672\n" \
  "ds_read_b64_tr_b16 v[94:95], v73 offset:4672\n" \
  "s_waitcnt lgkmcnt(12)\n" \
  "v_dot2c_f32_bf16 v76, 0x3f803f80, v96\n" \
  "v_dot2c_f32_bf16 v77, 0x3f803f80, v100\n" \
  "v_dot2c_f32_bf16 v78, 0x3f803f80, v104\n" \
  "v_dot2c_f32_bf16 v76, 0x3f803f80, v97\n" \
  "v_dot2c_f32_bf16 v77, 0x3f803f80, v101\n" \
  "v_dot2c_f32_bf16 v78, 0x3f803f80, v105\n" \
  "v_dot2c_f32_bf16 v76, 0x3f803f80, v98\n" \
  "v_dot2c_f32_bf16 v77, 0x3f803f80, v102\n" \
  "v_dot2c_f32_bf16 v78, 0x3f803f80, v106\n" \
  "v_dot2c_f32_bf16 v76, 0x3f803f80, v99\n" \
  "v_dot2c_f32_bf16 v77, 0x3f803f80, v103\n" \
  "v_dot2c_f32_bf16 v78, 0x3f803f80, v107\n" \
  "ds_read_b64_tr_b16 v[96:97], v72 offset:6144\n" \
  "ds_read_b64_tr_b16 v[98:99], v73 offset:6144\n" \
  "ds_read_b64_tr_b16 v[100:101], v72 offset:6176\n" \
  "ds_read_b64_tr_b16 v[102:103], v73 offset:6176\n" \
  "ds_read_b64_tr_b16 v[104:105], v72 offset:6208\n" \
  "ds_read_b64_tr_b16 v[106:107], v73 offset:6208\n" \
  "s_waitcnt lgkmcnt(12)\n" \
  "v_dot2c_f32_bf16 v76, 0x3f803f80, v108\n" \
  "v_dot2c_f32_bf16 v77, 0x3f803f80, v112\n" \
  "v_dot2c_f32_bf16 v78, 0x3f803f80, v116\n" \
  "v_dot2c_f32_bf16 v76, 0x3f803f80, v109\n" \
  "v_dot2c_f32_bf16 v77, 0x3f803f80, v113\n" \
  "v_dot2c_f32_bf16 v78, 0x3f803f80, v117\n" \
  "v_dot2c_f32_bf16 v76, 0x3f803f80, v110\n" \
  "v_dot2c_f32_bf16 v77, 0x3f803f80, v114\n" \
  "v_dot2c_f32_bf16 v78, 0x3f803f80, v118\n" \
  "v_dot2c_f32_bf16 v76, 0x3f803f80, v111\n" \
  "v_dot2c_f32_bf16 v77, 0x3f803f80, v115\n" \
  "v_dot2c_f32_bf16 v78, 0x3f803f80, v119\n" \
  "s_waitcnt vmcnt(0)\n" \
  "s_barrier\n" \
    \
  "s_cmp_lg_u32 s57, 0\n" \
  "s_cbranch_scc1 fbk_c9\n" \
  "s_mov_b32 exec_lo, 0x01010101\n" \
  "s_mov_b32 exec_hi, 0x01010101\n" \
  "s_xor_b32 s47, s61, 65536\n" \
  "buffer_load_dword v152, v251, s[12:15], s61 offen sc1\n" \
  "buffer_load_dword v153, v251, s[12:15], s47 offen sc1\n" \
  "s_mov_b64 exec, -1\n" \
  "fbk_c9:\n" \
  "ds_read_b64_tr_b16 v[108:109], v72 offset:7680\n" \
  "ds_read_b64_tr_b16 v[110:111], v73 offset:7680\n" \
  "ds_read_b64_tr_b16 v[112:113], v72 offset:7712\n" \
  "ds_read_b64_tr_b16 v[114:115], v73 offset:7712\n" \
  "ds_read_b64_tr_b16 v[116:117], v72 offset:7744\n" \
  "ds_read_b64_tr_b16 v[118:119], v73 offset:7744\n" \
  "s_waitcnt lgkmcnt(12)\n" \
  "v_dot2c_f32_bf16 v76, 0x3f803f80, v84\n" \
  "v_dot2c_f32_bf16 v77, 0x3f803f80, v88\n" \
  "v_dot2c_f32_bf16 v78, 0x3f803f80, v92\n" \
  "v_dot2c_f32_bf16 v76, 0x3f803f80, v85\n" \
  "v_dot2c_f32_bf16 v77, 0x3f803f80, v89\n" \
  "v_dot2c_f32_bf16 v78, 0x3f803f80, v93\n" \
  "v_dot2c_f32_bf16 v76, 0x3f803f80, v86\n" \
  "v_dot2c_f32_bf16 v77, 0x3f803f80, v90\n" \
  "v_dot2c_f32_bf16 v78, 0x3f803f80, v94\n" \
  "v_dot2c_f32_bf16 v76, 0x3f803f80, v87\n" \
  "v_dot2c_f32_bf16 v77, 0x3f803f80, v91\n" \
  "v_dot2c_f32_bf16 v78, 0x3f803f80, v95\n" \
  "ds_read_b64_tr_b16 v[84:85], v72 offset:9216\n" \
  "ds_read_b64_tr_b16 v[86:87], v73 offset:9216\n" \
  "ds_read_b64_tr_b16 v[88:89], v72 offset:9248\n" \
  "ds_read_b64_tr_b16 v[90:91], v73 offset:9248\n" \
  "ds_read_b64_tr_b16 v[92:93], v72 offset:9280\n" \
  "ds_read_b64_tr_b16 v[94:95], v73 offset:9280\n" \
  "s_waitcnt lgkmcnt(12)\n" \
  "v_dot2c_f32_bf16 v76, 0x3f803f80, v96\n" \
  "v_dot2c_f32_bf16 v77, 0x3f803f80, v100\n" \
  "v_dot2c_f32_bf16 v78, 0x3f803f80, v104\n" \
  "v_dot2c_f32_bf16 v76, 0x3f803f80, v97\n" \
  "v_dot2c_f32_bf16 v77, 0x3f803f80, v101\n" \
  "v_dot2c_f32_bf16 v78, 0x3f803f80, v105\n" \
  "v_dot2c_f32_bf16 v76, 0x3f803f80, v98\n" \
  "v_dot2c_f32_bf16 v77, 0x3f803f80, v102\n" \
  "v_dot2c_f32_bf16 v78, 0x3f803f80, v106\n" \
  "v_dot2c_f32_bf16 v76, 0x3f803f80, v99\n" \
  "v_dot2c_f32_bf16 v77, 0x3f803f80, v103\n" \
  "v_dot2c_f32_bf16 v78, 0x3f803f80, v107\n" \
  "ds_read_b64_tr_b16 v[96:97], v72 offset:10752\n" \
  "ds_read_b64_tr_b16 v[98:99], v73 offset:10752\n" \
  "ds_read_b64_tr_b16 v[100:101], v72 offset:10784\n" \
  "ds_read_b64_tr_b16 v[102:103], v73 offset:10784\n" \
  "ds_read_b64_tr_b16 v[104:105], v72 offset:10816\n" \
  "ds_read_b64_tr_b16 v[106:107], v73 offset:10816\n" \
  "s_waitcnt lgkmcnt(12)\n" \
  "v_dot2c_f32_bf16 v76, 0x3f803f80, v108\n" \
  "v_dot2c_f32_bf16 v77, 0x3f803f80, v112\n" \
  "v_dot2c_f32_bf16 v78, 0x3f803f80, v116\n" \
  "v_dot2c_f32_bf16 v76, 0x3f803f80, v109\n" \
  "v_dot2c_f32_bf16 v77, 0x3f803f80, v113\n" \
  "v_dot2c_f32_bf16 v78, 0x3f803f80, v117\n" \
  "v_dot2c_f32_bf16 v76, 0x3f803f80, v110\n" \
  "v_dot2c_f32_bf16 v77, 0x3f803f80, v114\n" \
  "v_dot2c_f32_bf16 v78, 0x3f803f80, v118\n" \
  "v_dot2c_f32_bf16 v76, 0x3f803f80, v111\n" \
  "v_dot2c_f32_bf16 v77, 0x3f803f80, v115\n" \
  "v_dot2c_f32_bf16 v78, 0x3f803f80, v119\n" \
  "s_waitcnt lgkmcnt(6)\n" \
  "v_dot2c_f32_bf16 v76, 0x3f803f80, v84\n" \
  "v_dot2c_f32_bf16 v77, 0x3f803f80, v88\n" \
  "v_dot2c_f32_bf16 v78, 0x3f803f80, v92\n" \
  "v_dot2c_f32_bf16 v76, 0x3f803f80, v85\n" \
  "v_dot2c_f32_bf16 v77, 0x3f803f80, v89\n" \
  "v_dot2c_f32_bf16 v78, 0x3f803f80, v93\n" \
  "v_dot2c_f32_bf16 v76, 0x3f803f80, v86\n" \
  "v_dot2c_f32_bf16 v77, 0x3f803f80, v90\n" \
  "v_dot2c_f32_bf16 v78, 0x3f803f80, v94\n" \
  "v_dot2c_f32_bf16 v76, 0x3f803f80, v87\n" \
  "v_dot2c_f32_bf16 v77, 0x3f803f80, v91\n" \
  "v_dot2c_f32_bf16 v78, 0x3f803f80, v95\n" \
  "s_waitcnt lgkmcnt(0)\n" \
  "v_dot2c_f32_bf16 v76, 0x3f803f80, v96\n" \
  "v_dot2c_f32_bf16 v77, 0x3f803f80, v100\n" \
  "v_dot2c_f32_bf16 v78, 0x3f803f80, v104\n" \
  "v_dot2c_f32_bf16 v76, 0x3f803f80, v97\n" \
  "v_dot2c_f32_bf16 v77, 0x3f803f80, v101\n" \
  "v_dot2c_f32_bf16 v78, 0x3f803f80, v105\n" \
  "v_dot2c_f32_bf16 v76, 0x3f803f80, v98\n" \
  "v_dot2c_f32_bf16 v77, 0x3f803f80, v102\n" \
  "v_dot2c_f32_bf16 v78, 0x3f803f80, v106\n" \
  "v_dot2c_f32_bf16 v76, 0x3f803f80, v99\n" \
  "v_dot2c_f32_bf16 v77, 0x3f803f80, v103\n" \
  "v_dot2c_f32_bf16 v78, 0x3f803f80, v107\n" \
  "s_branch fbk_c3\n" \
  "fbk_c1:\n" \
  "s_cmp_eq_u32 s36, 7\n" \
  "s_cbranch_scc1 fbk_c0\n" \
  "s_waitcnt vmcnt(0) lgkmcnt(0)\n" \
  "s_barrier\n" \
  "s_branch fbk_c3\n" \
  "fbk_c0:\n" \
    \
  "s_mov_b32 exec_lo, 0\n" \
  "s_mov_b32 exec_hi, 0xff\n" \
  "s_mov_b32 s47, 0\n" \
  "fbk_c2:\n" \
  "v_cmp_eq_u32 vcc, v249, v253\n" \
  "s_cbranch_vccz fbk_c2x\n" \
  "v_mov_b32 v249, 0\n" \
  "buffer_atomic_add v249, v252, s[12:15], 0 offen sc0\n" \
  "s_add_u32 s47, s47, 1\n" \
  "s_cmp_lt_u32 s47, 0x100000\n" \
  "s_waitcnt vmcnt(0)\n" \
  "s_cbranch_scc1 fbk_c2\n" \
  "fbk_c2x:\n" \
  "s_mov_b64 exec, -1\n" \
  "v_mov_b32 v149, 1\n" \
  "s_setprio 3\n" \
  "s_waitcnt vmcnt(0) lgkmcnt(0)\n" \
  "s_mov_b32 exec_lo, 0xffff\n" \
  "s_mov_b32 exec_hi, 0xff\n" \
  "s_barrier\n" \
  "buffer_atomic_add v149, v235, s[12:15], 0 offen\n" \
  "s_mov_b64 exec, -1\n" \
  "fbk_c3:\n" \
    \
    \
  "ds_write_b128 v64, v[200:203] offset:0\n" \
  "v_lshl_or_b32 v145, v144, 5, v145\n" \
  "v_and_b32 v146, 8, v250\n" \
  "v_lshl_or_b32 v145, v146, 3, v145\n" \
  "v_lshl_or_b32 v146, v142, 2, v143\n" \
  "v_lshl_or_b32 v244, v146, 7, v145\n" \
  "s_bfe_u32 s48, s36, 0x10001\n" \
  "s_lshl_b32 s47, s48, 15\n" \
  "s_add_u32 s47, s47, 98304\n" \
  "ds_write_b128 v64, v[204:207] offset:1024\n" \
  "v_add_u32 v66, s47, v244\n" \
  "v_xor_b32 v67, 64, v66\n" \
  "s_and_b32 s49, s36, 1\n" \
  "v_lshl_add_u32 v145, v142, 9, v251\n" \
  "v_lshl_add_u32 v145, s49, 9, v145\n" \
  "v_add_u32 v68, s47, v145\n" \
  "s_mul_i32 s47, s48, 17408\n" \
  "s_add_u32 s47, s47, 110592\n" \
  "ds_write_b128 v64, v[208:211] offset:2048\n" \
  "v_add_u32 v245, s47, v145\n" \
  "s_lshl1_add_u32 s47, s50, s49\n" \
  "s_mul_i32 s71, s47, 88064\n" \
  "s_add_u32 s71, s71, 0x800000\n" \
  "s_lshl_b32 s63, s45, 10\n" \
  "s_and_b32 s49, s37, 1\n" \
  "s_lshl1_add_u32 s47, s47, s49\n" \
  "s_lshl1_add_u32 s47, s47, s48\n" \
  "ds_write_b128 v64, v[212:215] offset:3072\n" \
  "s_lshl_b32 s47, s47, 13\n" \
  "s_add_u32 s67, s47, 0x400000\n" \
  "s_xor_b32 s68, s67, 4096\n" \
  "s_xor_b32 s69, s67, 16384\n" \
  "s_xor_b32 s70, s67, 20480\n" \
  "v_lshrrev_b32 v145, 4, v250\n" \
  "v_add_u32 v145, s36, v145\n" \
  "v_lshl_add_u32 v145, v145, 3, v140\n" \
  "ds_write_b128 v64, v[216:219] offset:4096\n" \
  "v_lshlrev_b32 v145, 4, v145\n" \
  "v_lshl_add_u32 v145, v141, 11, v145\n" \
  "v_add_u32 v69, 98304, v145\n" \
  "s_lshl1_add_u32 s48, s50, s46\n" \
  "s_lshl3_add_u32 s48, s48, s39\n" \
  "s_lshl_b32 s49, s48, 12\n" \
  "s_add_u32 s49, s49, 0x600000\n" \
  "s_lshl_b32 s48, s48, 8\n" \
  "ds_write_b128 v64, v[220:223] offset:5120\n" \
  "s_add_u32 s48, s48, 0xe00000\n" \
  "v_lshl_add_u32 v145, s45, 3, v140\n" \
  "v_lshl_add_u32 v249, v145, 3, s48\n" \
  "v_lshl_add_u32 v249, v144, 2, v249\n" \
  "v_lshl_add_u32 v145, v145, 4, s49\n" \
  "v_lshl_add_u32 v248, v141, 9, v145\n" \
  "v_lshlrev_b32 v70, 2, v141\n" \
  "v_lshl_or_b32 v70, v140, 5, v70\n" \
  "ds_write_b128 v64, v[224:227] offset:6144\n" \
  "s_lshl_b32 s48, s37, 8\n" \
  "s_add_u32 s48, s48, 0xf000a4\n" \
  "v_lshl_add_u32 v235, v250, 11, s48\n" \
  "s_lshl2_add_u32 s48, s45, 115712\n" \
  "v_add_u32 v71, s48, v251\n" \
  "v_lshlrev_b32 v236, 16, v176\n" \
  "v_and_b32 v237, 0xffff0000, v176\n" \
  "v_lshlrev_b32 v238, 16, v177\n" \
  "ds_write_b128 v64, v[228:231] offset:7168\n" \
  "v_and_b32 v239, 0xffff0000, v177\n" \
  "v_lshlrev_b32 v240, 16, v178\n" \
  "v_and_b32 v241, 0xffff0000, v178\n" \
  "v_lshlrev_b32 v242, 16, v179\n" \
  "v_and_b32 v243, 0xffff0000, v179\n" \
  "v_mov_b32 v80, 0\n" \
  "v_mov_b32 v81, 0\n" \
  "s_cmp_lg_u32 s36, 7\n" \
  "s_cbranch_scc1 fbk_c4\n" \
  "s_mov_b32 exec_lo, 0\n" \
  "s_mov_b32 exec_hi, 0x100\n" \
  "s_mov_b32 s47, 0\n" \
  "v_mov_b32 v148, 0\n" \
  "v_mov_b32 v149, 0\n" \
  "v_mov_b32 v150, 0\n" \
  "buffer_atomic_add v148, v252, s[12:15], 0 offen sc0\n" \
  "s_sleep 4\n" \
  "buffer_atomic_add v149, v252, s[12:15], 0 offen sc0\n" \
  "s_sleep 4\n" \
  "buffer_atomic_add v150, v252, s[12:15], 0 offen sc0\n" \
  "s_waitcnt vmcnt(2)\n" \
  "fbk_p2_loop:\n" \
  "v_sub_u32 v148, v148, v253\n" \
  "v_cmp_gt_u32 vcc, 16, v148\n" \
  "s_cbranch_vccz fbk_p2_ok\n" \
  "v_mov_b32 v148, 0\n" \
  "buffer_atomic_add v148, v252, s[12:15], 0 offen sc0\n" \
  "s_waitcnt vmcnt(2)\n" \
  "v_sub_u32 v149, v149, v253\n" \
  "v_cmp_gt_u32 vcc, 16, v149\n" \
  "s_cbranch_vccz fbk_p2_ok\n" \
  "v_mov_b32 v149, 0\n" \
  "buffer_atomic_add v149, v252, s[12:15], 0 offen sc0\n" \
  "s_waitcnt vmcnt(2)\n" \
  "v_sub_u32 v150, v150, v253\n" \
  "v_cmp_gt_u32 vcc, 16, v150\n" \
  "s_cbranch_vccz fbk_p2_ok\n" \
  "v_mov_b32 v150, 0\n" \
  "buffer_atomic_add v150, v252, s[12:15], 0 offen sc0\n" \
  "s_waitcnt vmcnt(2)\n" \
  "s_add_u32 s47, s47, 1\n" \
  "s_cmp_lt_u32 s47, 0x80000\n" \
  "s_cbranch_scc1 fbk_p2_loop\n" \
  "fbk_p2_ok:\n" \
  "s_waitcnt lgkmcnt(0)\n" \
  "s_barrier\n" \
  "s_mov_b64 exec, -1\n" \
  "s_setprio 0\n" \
  "s_waitcnt vmcnt(0)\n" \
  "s_branch fbk_c5\n" \
  "fbk_c4:\n" \
  "s_waitcnt lgkmcnt(0)\n" \
  "s_barrier\n" \
  "s_cmp_lt_u32 s36, 4\n" \
  "s_cbranch_scc1 fbk_c6\n" \
  "fbk_c5:\n" \
  "s_barrier\n" \
  "s_branch fbk_c7\n" \
  "fbk_c6:\n" \
  "buffer_load_dwordx4 v[140:143], v251, s[12:15], s67 offen offset:0 sc1\n" \
  "buffer_load_dwordx4 v[144:147], v251, s[12:15], s67 offen offset:1024 sc1\n" \
  "buffer_load_dwordx4 v[148:151], v251, s[12:15], s67 offen offset:2048 sc1\n" \
  "buffer_load_dwordx4 v[152:155], v251, s[12:15], s67 offen offset:3072 sc1\n" \
  "ds_read_b64_tr_b16 v[116:117], v66 offset:0\n" \
  "ds_read_b64_tr_b16 v[118:119], v66 offset:1024\n" \
  "ds_read_b64_tr_b16 v[120:121], v67 offset:0\n" \
  "ds_read_b64_tr_b16 v[122:123], v67 offset:1024\n" \
  "ds_read_b64_tr_b16 v[124:125], v66 offset:2048\n" \
  "ds_read_b64_tr_b16 v[126:127], v66 offset:3072\n" \
  "ds_read_b64_tr_b16 v[128:129], v67 offset:2048\n" \
  "ds_read_b64_tr_b16 v[130:131], v67 offset:3072\n" \
  "s_waitcnt vmcnt(3) lgkmcnt(4)\n" \
  "v_mfma_f32_32x32x16_bf16 v[84:99], v[116:119], v[140:143], 0\n" \
  "ds_read_b64_tr_b16 v[132:133], v66 offset:4096\n" \
  "ds_read_b64_tr_b16 v[134:135], v66 offset:5120\n" \
  "buffer_load_dwordx4 v[156:159], v251, s[12:15], s68 offen offset:0 sc1\n" \
  "v_mfma_f32_32x32x16_bf16 v[100:115], v[120:123], v[140:143], 0\n" \
  "ds_read_b64_tr_b16 v[136:137], v67 offset:4096\n" \
  "ds_read_b64_tr_b16 v[138:139], v67 offset:5120\n" \
  "buffer_load_dwordx4 v[160:163], v251, s[12:15], s68 offen offset:1024 sc1\n" \
  "s_waitcnt vmcnt(4) lgkmcnt(4)\n" \
  "v_mfma_f32_32x32x16_bf16 v[84:99], v[124:127], v[144:147], v[84:99]\n" \
  "ds_read_b64_tr_b16 v[116:117], v66 offset:6144\n" \
  "ds_read_b64_tr_b16 v[118:119], v66 offset:7168\n" \
  "buffer_load_dwordx4 v[164:167], v251, s[12:15], s68 offen offset:2048 sc1\n" \
  "v_mfma_f32_32x32x16_bf16 v[100:115], v[128:131], v[144:147], v[100:115]\n" \
  "ds_read_b64_tr_b16 v[120:121], v67 offset:6144\n" \
  "ds_read_b64_tr_b16 v[122:123], v67 offset:7168\n" \
  "buffer_load_dwordx4 v[168:171], v251, s[12:15], s68 offen offset:3072 sc1\n" \
  "s_waitcnt vmcnt(5) lgkmcnt(4)\n" \
  "v_mfma_f32_32x32x16_bf16 v[84:99], v[132:135], v[148:151], v[84:99]\n" \
  "ds_read_b64_tr_b16 v[124:125], v66 offset:8192\n" \
  "ds_read_b64_tr_b16 v[126:127], v66 offset:9216\n" \
  "buffer_load_dwordx4 v[172:175], v251, s[12:15], s69 offen offset:0 sc1\n" \
  "v_mfma_f32_32x32x16_bf16 v[100:115], v[136:139], v[148:151], v[100:115]\n" \
  "ds_read_b64_tr_b16 v[128:129], v67 offset:8192\n" \
  "ds_read_b64_tr_b16 v[130:131], v67 offset:9216\n" \
  "buffer_load_dwordx4 v[176:179], v251, s[12:15], s69 offen offset:1024 sc1\n" \
  "s_waitcnt vmcnt(6) lgkmcnt(4)\n" \
  "v_mfma_f32_32x32x16_bf16 v[84:99], v[116:119], v[152:155], v[84:99]\n" \
  "ds_read_b64_tr_b16 v[132:133], v66 offset:10240\n" \
  "ds_read_b64_tr_b16 v[134:135], v66 offset:11264\n" \
  "buffer_load_dwordx4 v[180:183], v251, s[12:15], s69 offen offset:2048 sc1\n" \
  "v_mfma_f32_32x32x16_bf16 v[100:115], v[120:123], v[152:155], v[100:115]\n" \
  "ds_read_b64_tr_b16 v[136:137], v67 offset:10240\n" \
  "ds_read_b64_tr_b16 v[138:139], v67 offset:11264\n" \
  "buffer_load_dwordx4 v[184:187], v251, s[12:15], s69 offen offset:3072 sc1\n" \
  "s_waitcnt vmcnt(7) lgkmcnt(4)\n" \
  "v_mfma_f32_32x32x16_bf16 v[84:99], v[124:127], v[156:159], v[84:99]\n" \
  "ds_read_b64_tr_b16 v[116:117], v66 offset:12288\n" \
  "ds_read_b64_tr_b16 v[118:119], v66 offset:13312\n" \
  "buffer_load_dwordx4 v[188:191], v251, s[12:15], s70 offen offset:0 sc1\n" \
  "v_mfma_f32_32x32x16_bf16 v[100:115], v[128:131], v[156:159], v[100:115]\n" \
  "ds_read_b64_tr_b16 v[120:121], v67 offset:12288\n" \
  "ds_read_b64_tr_b16 v[122:123], v67 offset:13312\n" \
  "buffer_load_dwordx4 v[192:195], v251, s[12:15], s70 offen offset:1024 sc1\n" \
  "s_waitcnt vmcnt(8) lgkmcnt(4)\n" \
  "v_mfma_f32_32x32x16_bf16 v[84:99], v[132:135], v[160:163], v[84:99]\n" \
  "ds_read_b64_tr_b16 v[124:125], v66 offset:14336\n" \
  "ds_read_b64_tr_b16 v[126:127], v66 offset:15360\n" \
  "buffer_load_dwordx4 v[196:199], v251, s[12:15], s70 offen offset:2048 sc1\n" \
  "v_mfma_f32_32x32x16_bf16 v[100:115], v[136:139], v[160:163], v[100:115]\n" \
  "ds_read_b64_tr_b16 v[128:129], v67 offset:14336\n" \
  "ds_read_b64_tr_b16 v[130:131], v67 offset:15360\n" \
  "buffer_load_dwordx4 v[200:203], v251, s[12:15], s70 offen offset:3072 sc1\n" \
  "s_waitcnt vmcnt(9) lgkmcnt(4)\n" \
  "v_mfma_f32_32x32x16_bf16 v[84:99], v[116:119], v[164:167], v[84:99]\n" \
  "ds_read_b64_tr_b16 v[132:133], v66 offset:16384\n" \
  "ds_read_b64_tr_b16 v[134:135], v66 offset:17408\n" \
  "v_mfma_f32_32x32x16_bf16 v[100:115], v[120:123], v[164:167], v[100:115]\n" \
  "ds_read_b64_tr_b16 v[136:137], v67 offset:16384\n" \
  "ds_read_b64_tr_b16 v[138:139], v67 offset:17408\n" \
  "s_waitcnt vmcnt(8) lgkmcnt(4)\n" \
  "v_mfma_f32_32x32x16_bf16 v[84:99], v[124:127], v[168:171], v[84:99]\n" \
  "ds_read_b64_tr_b16 v[116:117], v66 offset:18432\n" \
  "ds_read_b64_tr_b16 v[118:119], v66 offset:19456\n" \
  "v_mfma_f32_32x32x16_bf16 v[100:115], v[128:131], v[168:171], v[100:115]\n" \
  "ds_read_b64_tr_b16 v[120:121], v67 offset:18432\n" \
  "ds_read_b64_tr_b16 v[122:123], v67 offset:19456\n" \
  "s_waitcnt vmcnt(7) lgkmcnt(4)\n" \
  "v_mfma_f32_32x32x16_bf16 v[84:99], v[132:135], v[172:175], v[84:99]\n" \
  "ds_read_b64_tr_b16 v[124:125], v66 offset:20480\n" \
  "ds_read_b64_tr_b16 v[126:127], v66 offset:21504\n" \
  "v_mfma_f32_32x32x16_bf16 v[100:115], v[136:139], v[172:175], v[100:115]\n" \
  "ds_read_b64_tr_b16 v[128:129], v67 offset:20480\n" \
  "ds_read_b64_tr_b16 v[130:131], v67 offset:21504\n" \
  "s_waitcnt vmcnt(6) lgkmcnt(4)\n" \
  "v_mfma_f32_32x32x16_bf16 v[84:99], v[116:119], v[176:179], v[84:99]\n" \
  "ds_read_b64_tr_b16 v[132:133], v66 offset:22528\n" \
  "ds_read_b64_tr_b16 v[134:135], v66 offset:23552\n" \
  "v_mfma_f32_32x32x16_bf16 v[100:115], v[120:123], v[176:179], v[100:115]\n" \
  "ds_read_b64_tr_b16 v[136:137], v67 offset:22528\n" \
  "ds_read_b64_tr_b16 v[138:139], v67 offset:23552\n" \
    \
  "s_barrier\n" \
  "s_waitcnt vmcnt(5) lgkmcnt(4)\n" \
  "v_mfma_f32_32x32x16_bf16 v[84:99], v[124:127], v[180:183], v[84:99]\n" \
  "ds_read_b64_tr_b16 v[116:117], v66 offset:24576\n" \
  "ds_read_b64_tr_b16 v[118:119], v66 offset:25600\n" \
  "v_mfma_f32_32x32x16_bf16 v[100:115], v[128:131], v[180:183], v[100:115]\n" \
  "ds_read_b64_tr_b16 v[120:121], v67 offset:24576\n" \
  "ds_read_b64_tr_b16 v[122:123], v67 offset:25600\n" \
  "s_waitcnt vmcnt(4) lgkmcnt(4)\n" \
  "v_mfma_f32_32x32x16_bf16 v[84:99], v[132:135], v[184:187], v[84:99]\n" \
  "ds_read_b64_tr_b16 v[124:125], v66 offset:26624\n" \
  "ds_read_b64_tr_b16 v[126:127], v66 offset:27648\n" \
  "v_mfma_f32_32x32x16_bf16 v[100:115], v[136:139], v[184:187], v[100:115]\n" \
  "ds_read_b64_tr_b16 v[128:129], v67 offset:26624\n" \
  "ds_read_b64_tr_b16 v[130:131], v67 offset:27648\n" \
  "s_waitcnt vmcnt(3) lgkmcnt(4)\n" \
  "v_mfma_f32_32x32x16_bf16 v[84:99], v[116:119], v[188:191], v[84:99]\n" \
  "ds_read_b64_tr_b16 v[132:133], v66 offset:28672\n" \
  "ds_read_b64_tr_b16 v[134:135], v66 offset:29696\n" \
  "v_mfma_f32_32x32x16_bf16 v[100:115], v[120:123], v[188:191], v[100:115]\n" \
  "ds_read_b64_tr_b16 v[136:137], v67 offset:28672\n" \
  "ds_read_b64_tr_b16 v[138:139], v67 offset:29696\n" \
  "s_waitcnt vmcnt(2) lgkmcnt(4)\n" \
  "v_mfma_f32_32x32x16_bf16 v[84:99], v[124:127], v[192:195], v[84:99]\n" \
  "ds_read_b64_tr_b16 v[116:117], v66 offset:30720\n" \
  "ds_read_b64_tr_b16 v[118:119], v66 offset:31744\n" \
  "v_mfma_f32_32x32x16_bf16 v[100:115], v[128:131], v[192:195], v[100:115]\n" \
  "ds_read_b64_tr_b16 v[120:121], v67 offset:30720\n" \
  "ds_read_b64_tr_b16 v[122:123], v67 offset:31744\n" \
  "ds_write_b32 v71, v76 offset:0\n" \
  "s_waitcnt vmcnt(1) lgkmcnt(5)\n" \
  "v_mfma_f32_32x32x16_bf16 v[84:99], v[132:135], v[196:199], v[84:99]\n" \
  "ds_write_b32 v71, v77 offset:1024\n" \
  "v_mfma_f32_32x32x16_bf16 v[100:115], v[136:139], v[196:199], v[100:115]\n" \
  "ds_write_b32 v71, v78 offset:2048\n" \
  "s_waitcnt vmcnt(0) lgkmcnt(3)\n" \
  "v_mfma_f32_32x32x16_bf16 v[84:99], v[116:119], v[200:203], v[84:99]\n" \
  "v_mfma_f32_32x32x16_bf16 v[100:115], v[120:123], v[200:203], v[100:115]\n" \
  "s_nop 10\n" \
  "ds_write_b128 v68, v[84:87] offset:0\n" \
  "ds_write_b128 v68, v[88:91] offset:2048\n" \
  "ds_write_b128 v68, v[92:95] offset:4224\n" \
  "ds_write_b128 v68, v[96:99] offset:6272\n" \
  "ds_write_b128 v68, v[100:103] offset:8448\n" \
  "ds_write_b128 v68, v[104:107] offset:10496\n" \
  "ds_write_b128 v68, v[108:111] offset:12672\n" \
  "ds_write_b128 v68, v[112:115] offset:14720\n" \
  "fbk_c7:\n" \
  "s_waitcnt lgkmcnt(0)\n" \
  "s_barrier\n" \
  "ds_read_b128 v[152:155], v69\n" \
  "ds_read_b128 v[160:163], v69 offset:32768\n" \
  "ds_read_b128 v[156:159], v69 offset:1024\n" \
  "ds_read_b128 v[164:167], v69 offset:33792\n" \
  "s_waitcnt lgkmcnt(3)\n" \
  "v_pk_add_f32 v[236:237], v[236:237], v[152:153]\n" \
  "v_pk_add_f32 v[238:239], v[238:239], v[154:155]\n" \
  "s_waitcnt lgkmcnt(2)\n" \
  "v_pk_add_f32 v[236:237], v[236:237], v[160:161]\n" \
  "v_pk_add_f32 v[238:239], v[238:239], v[162:163]\n" \
  "s_waitcnt lgkmcnt(1)\n" \
  "v_pk_add_f32 v[240:241], v[240:241], v[156:157]\n" \
  "v_pk_add_f32 v[242:243], v[242:243], v[158:159]\n" \
  "s_waitcnt lgkmcnt(0)\n" \
  "v_pk_add_f32 v[240:241], v[240:241], v[164:165]\n" \
  "v_pk_add_f32 v[242:243], v[242:243], v[166:167]\n" \
  "v_cvt_pk_bf16_f32 v168, v236, v237\n" \
  "v_cvt_pk_bf16_f32 v169, v238, v239\n" \
  "v_cvt_pk_bf16_f32 v170, v240, v241\n" \
  "v_cvt_pk_bf16_f32 v171, v242, v243\n" \
  "buffer_store_dwordx4 v[168:171], v248, s[12:15], 0 offen sc1\n" \
    \
  "v_dot2c_f32_bf16 v80, 0x3f803f80, v168\n" \
  "v_dot2c_f32_bf16 v81, v168, v168\n" \
  "v_dot2c_f32_bf16 v80, 0x3f803f80, v169\n" \
  "v_dot2c_f32_bf16 v81, v169, v169\n" \
  "v_dot2c_f32_bf16 v80, 0x3f803f80, v170\n" \
  "v_dot2c_f32_bf16 v81, v170, v170\n" \
  "v_dot2c_f32_bf16 v80, 0x3f803f80, v171\n" \
  "v_dot2c_f32_bf16 v81, v171, v171\n" \
  "s_nop 2\n" \
  "v_permlane16_swap_b32 v80, v81\n" \
  "v_add_f32 v80, v80, v81\n" \
  "v_mov_b32 v81, v80\n" \
  "s_nop 1\n" \
  "v_permlane32_swap_b32 v80, v81\n" \
  "v_add_f32 v80, v80, v81\n" \
  "s_nop 1\n" \
  "v_add_f32_dpp v80, v80, v80 row_ror:8 row_mask:0xf bank_mask:0xf\n" \
  "s_mov_b64 exec, 0xff00ff\n" \
  "buffer_store_dword v80, v249, s[12:15], 0 offen sc1\n" \
  "s_mov_b64 exec, -1\n" \
  "v_add_u32 v65, 115712, v251\n" \
  "ds_read_b128 v[204:207], v65 offset:0\n" \
  "ds_read_b128 v[208:211], v65 offset:1024\n" \
  "ds_read_b128 v[212:215], v65 offset:2048\n" \
    \
  "s_and_b32 s50, s38, 1\n" \
  "s_lshl_b32 s48, s50, 2\n" \
  "s_xor_b32 s48, s48, s36\n" \
  "s_lshl3_add_u32 s49, s37, s48\n" \
  "s_lshl_b32 s48, s49, 11\n" \
  "s_add_u32 s48, s48, 0xe00000\n" \
  "v_lshl_add_u32 v64, v250, 3, s48\n" \
  "s_and_b32 s48, s38, 3\n" \
  "s_lshl2_add_u32 s64, s49, s48\n" \
  "s_lshl_b32 s64, s64, 13\n" \
  "s_add_u32 s64, s64, 0x600000\n" \
  "s_mul_i32 s65, s49, 88064\n" \
  "s_mul_i32 s48, s38, 3072\n" \
  "s_add_u32 s65, s65, s48\n" \
  "s_add_u32 s65, s65, 0x800000\n" \
    \
  "s_lshl_b32 s48, s50, 4\n" \
  "s_lshl_b32 s68, 0xffff, s48\n" \
  "s_cmp_ge_u32 s38, 28\n" \
  "s_cselect_b32 s68, 0, s68\n" \
  "s_not_b32 s69, s68\n" \
  "s_cmp_ge_u32 s36, 4\n" \
  "s_cbranch_scc1 fbk_d13\n" \
    \
  "v_add_u32 v224, s63, v251\n" \
  "v_add_u32 v225, 65536, v224\n" \
  "v_add_u32 v226, 131072, v224\n" \
  "ds_write_b128 v225, v[20:23] offset:53248\n" \
  "ds_write_b128 v225, v[24:27] offset:57344\n" \
  "ds_write_b128 v225, v[28:31] offset:61440\n" \
  "ds_write_b128 v226, v[52:55] offset:20480\n" \
  "ds_write_b128 v226, v[56:59] offset:24576\n" \
  "ds_write_b128 v226, v[60:63] offset:28672\n" \
  "fbk_d13:\n" \
  "s_cmp_eq_u32 s36, 7\n" \
  "s_cbranch_scc1 fbk_d0\n" \
  "s_waitcnt vmcnt(0) lgkmcnt(0)\n" \
  "s_barrier\n" \
  "s_cmp_ge_u32 s36, 4\n" \
  "s_cbranch_scc1 fbk_d2\n" \
    \
  "s_sleep 1\n" \
  "s_mov_b32 exec_lo, 0x01010101\n" \
  "s_mov_b32 exec_hi, 0x01010101\n" \
  "s_mov_b32 s48, 32768\n" \
  "buffer_load_dword v220, v248, s[12:15], 0 offen sc1\n" \
  "buffer_load_dword v221, v248, s[12:15], s48 offen sc1\n" \
  "s_mov_b64 exec, 1\n" \
  "buffer_load_dword v222, v249, s[12:15], 0 offen sc1\n" \
  "buffer_load_dword v223, v249, s[12:15], 0 offen offset:2048 sc1\n" \
  "s_mov_b64 exec, -1\n" \
  "s_branch fbk_d2\n" \
  "fbk_d0:\n" \
  "v_mov_b32 v220, 1\n" \
  "s_setprio 3\n" \
  "s_waitcnt vmcnt(0) lgkmcnt(0)\n" \
  "s_mov_b32 exec_lo, 0x1fffffff\n" \
  "s_mov_b32 exec_hi, 0\n" \
  "s_barrier\n" \
  "buffer_atomic_add v220, v235, s[12:15], 0 offen\n" \
  "s_mov_b64 exec, -1\n" \
  "v_add_u32 v235, 4, v235\n" \
  "fbk_d2:\n" \
    \
  "ds_bpermute_b32 v236, v70, v236\n" \
  "ds_bpermute_b32 v237, v70, v237\n" \
  "ds_bpermute_b32 v238, v70, v238\n" \
  "ds_bpermute_b32 v239, v70, v239\n" \
  "ds_bpermute_b32 v240, v70, v240\n" \
  "ds_bpermute_b32 v241, v70, v241\n" \
  "ds_bpermute_b32 v242, v70, v242\n" \
  "ds_bpermute_b32 v243, v70, v243\n" \
    \
  "ds_read_b64_tr_b16 v[128:129], v246 offset:0\n" \
  "ds_read_b64_tr_b16 v[130:131], v247 offset:0\n" \
  "ds_read_b64_tr_b16 v[132:133], v246 offset:32\n" \
  "ds_read_b64_tr_b16 v[134:135], v247 offset:32\n" \
  "ds_read_b64_tr_b16 v[136:137], v246 offset:64\n" \
  "ds_read_b64_tr_b16 v[138:139], v247 offset:64\n" \
  "ds_read_b64_tr_b16 v[140:141], v246 offset:1536\n" \
  "ds_read_b64_tr_b16 v[142:143], v247 offset:1536\n" \
  "ds_read_b64_tr_b16 v[144:145], v246 offset:1568\n" \
  "ds_read_b64_tr_b16 v[146:147], v247 offset:1568\n" \
  "ds_read_b64_tr_b16 v[148:149], v246 offset:1600\n" \
  "ds_read_b64_tr_b16 v[150:151], v247 offset:1600\n" \
  "v_pk_add_f32 v[204:205], v[204:205], v[206:207]\n" \
  "v_pk_add_f32 v[208:209], v[208:209], v[210:211]\n" \
  "v_pk_add_f32 v[212:213], v[212:213], v[214:215]\n" \
  "v_add_f32 v204, v204, v205\n" \
  "v_add_f32 v208, v208, v209\n" \
  "v_add_f32 v212, v212, v213\n" \
  "v_cvt_pk_bf16_f32 v72, v204, v204\n" \
  "v_and_b32 v205, 0xffff0000, v72\n" \
  "v_sub_f32 v205, v204, v205\n" \
  "v_cvt_pk_bf16_f32 v73, v205, v205\n" \
  "v_cvt_pk_bf16_f32 v74, v208, v208\n" \
  "v_and_b32 v209, 0xffff0000, v74\n" \
  "v_sub_f32 v209, v208, v209\n" \
  "v_cvt_pk_bf16_f32 v75, v209, v209\n" \
  "v_cvt_pk_bf16_f32 v76, v212, v212\n" \
  "v_and_b32 v213, 0xffff0000, v76\n" \
  "v_sub_f32 v213, v212, v213\n" \
  "v_cvt_pk_bf16_f32 v77, v213, v213\n" \
  "s_cmp_lg_u32 s66, 7\n" \
  "s_cbranch_scc1 fbk_d12\n" \
  "s_mov_b32 exec_lo, 0\n" \
  "s_mov_b32 exec_hi, 0x200\n" \
  "s_mov_b32 s47, 0\n" \
  "v_mov_b32 v204, 0\n" \
  "v_mov_b32 v205, 0\n" \
  "v_mov_b32 v206, 0\n" \
  "buffer_atomic_add v204, v252, s[12:15], 0 offen sc0\n" \
  "s_sleep 4\n" \
  "buffer_atomic_add v205, v252, s[12:15], 0 offen sc0\n" \
  "s_sleep 4\n" \
  "buffer_atomic_add v206, v252, s[12:15], 0 offen sc0\n" \
  "s_waitcnt vmcnt(2)\n" \
  "fbk_p3_loop:\n" \
  "v_sub_u32 v204, v204, v253\n" \
  "v_cmp_gt_u32 vcc, 32, v204\n" \
  "s_cbranch_vccz fbk_p3_ok\n" \
  "v_mov_b32 v204, 0\n" \
  "buffer_atomic_add v204, v252, s[12:15], 0 offen sc0\n" \
  "s_waitcnt vmcnt(2)\n" \
  "v_sub_u32 v205, v205, v253\n" \
  "v_cmp_gt_u32 vcc, 32, v205\n" \
  "s_cbranch_vccz fbk_p3_ok\n" \
  "v_mov_b32 v205, 0\n" \
  "buffer_atomic_add v205, v252, s[12:15], 0 offen sc0\n" \
  "s_waitcnt vmcnt(2)\n" \
  "v_sub_u32 v206, v206, v253\n" \
  "v_cmp_gt_u32 vcc, 32, v206\n" \
  "s_cbranch_vccz fbk_p3_ok\n" \
  "v_mov_b32 v206, 0\n" \
  "buffer_atomic_add v206, v252, s[12:15], 0 offen sc0\n" \
  "s_waitcnt vmcnt(2)\n" \
  "s_add_u32 s47, s47, 1\n" \
  "s_cmp_lt_u32 s47, 0x80000\n" \
  "s_cbranch_scc1 fbk_p3_loop\n" \
  "fbk_p3_ok:\n" \
  "s_waitcnt lgkmcnt(0)\n" \
  "s_barrier\n" \
  "s_mov_b64 exec, -1\n" \
  "s_setprio 0\n" \
  "s_waitcnt vmcnt(0)\n" \
  "s_branch fbk_d3\n" \
  "fbk_d12:\n" \
  "s_cmp_ge_u32 s36, 4\n" \
  "s_cbranch_scc1 fbk_d1\n" \
    \
  "ds_write_b128 v225, v[0:3] offset:32768\n" \
  "ds_write_b128 v225, v[4:7] offset:36864\n" \
  "ds_write_b128 v225, v[8:11] offset:40960\n" \
  "ds_write_b128 v225, v[12:15] offset:45056\n" \
  "ds_write_b128 v225, v[16:19] offset:49152\n" \
  "ds_write_b128 v226, v[32:35] offset:0\n" \
  "ds_write_b128 v226, v[36:39] offset:4096\n" \
  "ds_write_b128 v226, v[40:43] offset:8192\n" \
  "ds_write_b128 v226, v[44:47] offset:12288\n" \
  "ds_write_b128 v226, v[48:51] offset:16384\n" \
  "s_branch fbk_d1\n" \
  ".p2align 12\n" \
  "fbk_d1:\n" \
  "s_waitcnt lgkmcnt(0)\n" \
  "s_barrier\n" \
    \
  "s_cmp_ge_u32 s66, 8\n" \
  "s_cbranch_scc1 fbk_d6\n" \
  "s_setprio 2\n" \
  "s_cmp_lt_u32 s36, 4\n" \
  "s_cbranch_scc1 fbk_d4\n" \
  "fbk_d3:\n" \
    \
  "s_setprio 1\n" \
  "s_mov_b32 s26, 0\n" \
  "s_sleep 24\n" \
  "s_xor_b32 s48, s64, 0\n" \
  "buffer_load_dwordx4 v[160:163], v251, s[12:15], s48 offen offset:0 sc1\n" \
  "buffer_load_dwordx4 v[164:167], v251, s[12:15], s48 offen offset:1024 sc1\n" \
  "s_sleep 3\n" \
  "buffer_load_dwordx2 v[212:213], v64, s[12:15], 0 offen offset:0 sc1\n" \
  "buffer_load_dwordx2 v[214:215], v64, s[12:15], 0 offen offset:512 sc1\n" \
  "s_sleep 3\n" \
  "buffer_load_dwordx2 v[216:217], v64, s[12:15], 0 offen offset:1024 sc1\n" \
  "buffer_load_dwordx2 v[218:219], v64, s[12:15], 0 offen offset:1536 sc1\n" \
  "s_sleep 3\n" \
  "buffer_load_dwordx4 v[168:171], v251, s[12:15], s48 offen offset:2048 sc1\n" \
  "s_sleep 3\n" \
  "buffer_load_dwordx4 v[172:175], v251, s[12:15], s48 offen offset:3072 sc1\n" \
  "s_sleep 3\n" \
  "s_xor_b32 s48, s64, 4096\n" \
  "buffer_load_dwordx4 v[176:179], v251, s[12:15], s48 offen offset:0 sc1\n" \
  "s_barrier\n" \
  "s_branch fbk_d5\n" \
  "fbk_d4:\n" \
  "s_xor_b32 s48, s64, 0\n" \
  "buffer_load_dwordx4 v[160:163], v251, s[12:15], s48 offen offset:0 sc1\n" \
  "buffer_load_dwordx4 v[164:167], v251, s[12:15], s48 offen offset:1024 sc1\n" \
  "buffer_load_dwordx2 v[212:213], v64, s[12:15], 0 offen offset:0 sc1\n" \
  "buffer_load_dwordx2 v[214:215], v64, s[12:15], 0 offen offset:512 sc1\n" \
  "buffer_load_dwordx2 v[216:217], v64, s[12:15], 0 offen offset:1024 sc1\n" \
  "buffer_load_dwordx2 v[218:219], v64, s[12:15], 0 offen offset:1536 sc1\n" \
  "buffer_load_dwordx4 v[168:171], v251, s[12:15], s48 offen offset:2048 sc1\n" \
  "buffer_load_dwordx4 v[172:175], v251, s[12:15], s48 offen offset:3072 sc1\n" \
  "s_xor_b32 s48, s64, 4096\n" \
  "buffer_load_dwordx4 v[176:179], v251, s[12:15], s48 offen offset:0 sc1\n" \
  "fbk_d5:\n" \
  "s_waitcnt vmcnt(8)\n" \
  "v_mfma_f32_32x32x16_bf16 v[80:95], v[128:131], v[160:163], 0\n" \
  "ds_read_b64_tr_b16 v[152:153], v246 offset:3072\n" \
  "ds_read_b64_tr_b16 v[154:155], v247 offset:3072\n" \
  "buffer_load_dwordx4 v[180:183], v251, s[12:15], s48 offen offset:1024 sc1\n" \
  "v_mfma_f32_32x32x16_bf16 v[96:111], v[132:135], v[160:163], 0\n" \
  "ds_read_b64_tr_b16 v[156:157], v246 offset:3104\n" \
  "ds_read_b64_tr_b16 v[158:159], v247 offset:3104\n" \
  "buffer_load_dwordx4 v[184:187], v251, s[12:15], s48 offen offset:2048 sc1\n" \
  "v_mfma_f32_32x32x16_bf16 v[112:127], v[136:139], v[160:163], 0\n" \
  "ds_read_b64_tr_b16 v[128:129], v246 offset:3136\n" \
  "ds_read_b64_tr_b16 v[130:131], v247 offset:3136\n" \
  "s_waitcnt vmcnt(9)\n" \
  "v_mfma_f32_32x32x16_bf16 v[80:95], v[140:143], v[164:167], v[80:95]\n" \
  "ds_read_b64_tr_b16 v[132:133], v246 offset:4608\n" \
  "ds_read_b64_tr_b16 v[134:135], v247 offset:4608\n" \
  "buffer_load_dwordx4 v[188:191], v251, s[12:15], s48 offen offset:3072 sc1\n" \
  "v_mfma_f32_32x32x16_bf16 v[96:111], v[144:147], v[164:167], v[96:111]\n" \
  "ds_read_b64_tr_b16 v[136:137], v246 offset:4640\n" \
  "ds_read_b64_tr_b16 v[138:139], v247 offset:4640\n" \
  "s_xor_b32 s48, s64, 8192\n" \
  "buffer_load_dwordx4 v[192:195], v251, s[12:15], s48 offen offset:0 sc1\n" \
  "v_mfma_f32_32x32x16_bf16 v[112:127], v[148:151], v[164:167], v[112:127]\n" \
  "ds_read_b64_tr_b16 v[140:141], v246 offset:4672\n" \
  "ds_read_b64_tr_b16 v[142:143], v247 offset:4672\n" \
  "s_waitcnt vmcnt(6) lgkmcnt(6)\n" \
  "v_mfma_f32_32x32x16_bf16 v[80:95], v[152:155], v[168:171], v[80:95]\n" \
  "ds_read_b64_tr_b16 v[144:145], v246 offset:6144\n" \
  "ds_read_b64_tr_b16 v[146:147], v247 offset:6144\n" \
  "buffer_load_dwordx4 v[196:199], v251, s[12:15], s48 offen offset:1024 sc1\n" \
  "v_mfma_f32_32x32x16_bf16 v[96:111], v[156:159], v[168:171], v[96:111]\n" \
  "ds_read_b64_tr_b16 v[148:149], v246 offset:6176\n" \
  "ds_read_b64_tr_b16 v[150:151], v247 offset:6176\n" \
  "buffer_load_dwordx4 v[200:203], v251, s[12:15], s48 offen offset:2048 sc1\n" \
  "v_mfma_f32_32x32x16_bf16 v[112:127], v[128:131], v[168:171], v[112:127]\n" \
  "ds_read_b64_tr_b16 v[152:153], v246 offset:6208\n" \
  "ds_read_b64_tr_b16 v[154:155], v247 offset:6208\n" \
  "s_waitcnt vmcnt(7) lgkmcnt(6)\n" \
  "v_mfma_f32_32x32x16_bf16 v[80:95], v[132:135], v[172:175], v[80:95]\n" \
  "ds_read_b64_tr_b16 v[156:157], v246 offset:7680\n" \
  "ds_read_b64_tr_b16 v[158:159], v247 offset:7680\n" \
  "buffer_load_dwordx4 v[160:163], v251, s[12:15], s48 offen offset:3072 sc1\n" \
  "v_mfma_f32_32x32x16_bf16 v[96:111], v[136:139], v[172:175], v[96:111]\n" \
  "ds_read_b64_tr_b16 v[128:129], v246 offset:7712\n" \
  "ds_read_b64_tr_b16 v[130:131], v247 offset:7712\n" \
  "s_xor_b32 s48, s64, 12288\n" \
  "buffer_load_dwordx4 v[164:167], v251, s[12:15], s48 offen offset:0 sc1\n" \
  "v_mfma_f32_32x32x16_bf16 v[112:127], v[140:143], v[172:175], v[112:127]\n" \
  "ds_read_b64_tr_b16 v[132:133], v246 offset:7744\n" \
  "ds_read_b64_tr_b16 v[134:135], v247 offset:7744\n" \
  "s_waitcnt vmcnt(8) lgkmcnt(6)\n" \
  "v_mfma_f32_32x32x16_bf16 v[80:95], v[144:147], v[176:179], v[80:95]\n" \
  "ds_read_b64_tr_b16 v[136:137], v246 offset:9216\n" \
  "ds_read_b64_tr_b16 v[138:139], v247 offset:9216\n" \
  "buffer_load_dwordx4 v[168:171], v251, s[12:15], s48 offen offset:1024 sc1\n" \
  "v_mfma_f32_32x32x16_bf16 v[96:111], v[148:151], v[176:179], v[96:111]\n" \
  "ds_read_b64_tr_b16 v[140:141], v246 offset:9248\n" \
  "ds_read_b64_tr_b16 v[142:143], v247 offset:9248\n" \
  "buffer_load_dwordx4 v[172:175], v251, s[12:15], s48 offen offset:2048 sc1\n" \
  "v_mfma_f32_32x32x16_bf16 v[112:127], v[152:155], v[176:179], v[112:127]\n" \
  "ds_read_b64_tr_b16 v[144:145], v246 offset:9280\n" \
  "ds_read_b64_tr_b16 v[146:147], v247 offset:9280\n" \
  "s_waitcnt vmcnt(9) lgkmcnt(6)\n" \
  "v_mfma_f32_32x32x16_bf16 v[80:95], v[156:159], v[180:183], v[80:95]\n" \
  "ds_read_b64_tr_b16 v[148:149], v246 offset:10752\n" \
  "ds_read_b64_tr_b16 v[150:151], v247 offset:10752\n" \
  "buffer_load_dwordx4 v[176:179], v251, s[12:15], s48 offen offset:3072 sc1\n" \
  "v_mfma_f32_32x32x16_bf16 v[96:111], v[128:131], v[180:183], v[96:111]\n" \
  "ds_read_b64_tr_b16 v[152:153], v246 offset:10784\n" \
  "ds_read_b64_tr_b16 v[154:155], v247 offset:10784\n" \
  "v_mfma_f32_32x32x16_bf16 v[112:127], v[132:135], v[180:183], v[112:127]\n" \
  "ds_read_b64_tr_b16 v[156:157], v246 offset:10816\n" \
  "ds_read_b64_tr_b16 v[158:159], v247 offset:10816\n" \
  "s_waitcnt vmcnt(9) lgkmcnt(6)\n" \
  "v_mfma_f32_32x32x16_bf16 v[80:95], v[136:139], v[184:187], v[80:95]\n" \
  "ds_read_b64_tr_b16 v[128:129], v246 offset:12288\n" \
  "ds_read_b64_tr_b16 v[130:131], v247 offset:12288\n" \
  "s_xor_b32 s48, s64, 16384\n" \
  "buffer_load_dwordx4 v[180:183], v251, s[12:15], s48 offen offset:0 sc1\n" \
  "v_mfma_f32_32x32x16_bf16 v[96:111], v[140:143], v[184:187], v[96:111]\n" \
  "ds_read_b64_tr_b16 v[132:133], v246 offset:12320\n" \
  "ds_read_b64_tr_b16 v[134:135], v247 offset:12320\n" \
  "v_add_f32 v66, v212, v214\n" \
  "v_add_f32 v67, v213, v215\n" \
  "v_mfma_f32_32x32x16_bf16 v[112:127], v[144:147], v[184:187], v[112:127]\n" \
  "ds_read_b64_tr_b16 v[136:137], v246 offset:12352\n" \
  "ds_read_b64_tr_b16 v[138:139], v247 offset:12352\n" \
  "s_waitcnt vmcnt(9) lgkmcnt(6)\n" \
  "v_mfma_f32_32x32x16_bf16 v[80:95], v[148:151], v[188:191], v[80:95]\n" \
  "ds_read_b64_tr_b16 v[140:141], v246 offset:13824\n" \
  "ds_read_b64_tr_b16 v[142:143], v247 offset:13824\n" \
  "buffer_load_dwordx4 v[184:187], v251, s[12:15], s48 offen offset:1024 sc1\n" \
  "v_mfma_f32_32x32x16_bf16 v[96:111], v[152:155], v[188:191], v[96:111]\n" \
  "ds_read_b64_tr_b16 v[144:145], v246 offset:13856\n" \
  "ds_read_b64_tr_b16 v[146:147], v247 offset:13856\n" \
  "v_add_f32 v204, v216, v218\n" \
  "v_add_f32 v205, v217, v219\n" \
  "v_mfma_f32_32x32x16_bf16 v[112:127], v[156:159], v[188:191], v[112:127]\n" \
  "ds_read_b64_tr_b16 v[148:149], v246 offset:13888\n" \
  "ds_read_b64_tr_b16 v[150:151], v247 offset:13888\n" \
  "s_waitcnt vmcnt(9) lgkmcnt(6)\n" \
  "v_mfma_f32_32x32x16_bf16 v[80:95], v[128:131], v[192:195], v[80:95]\n" \
  "ds_read_b64_tr_b16 v[152:153], v246 offset:15360\n" \
  "ds_read_b64_tr_b16 v[154:155], v247 offset:15360\n" \
  "buffer_load_dwordx4 v[188:191], v251, s[12:15], s48 offen offset:2048 sc1\n" \
  "v_mfma_f32_32x32x16_bf16 v[96:111], v[132:135], v[192:195], v[96:111]\n" \
  "ds_read_b64_tr_b16 v[156:157], v246 offset:15392\n" \
  "ds_read_b64_tr_b16 v[158:159], v247 offset:15392\n" \
  "v_add_f32 v66, v66, v204\n" \
  "v_add_f32 v67, v67, v205\n" \
  "v_mfma_f32_32x32x16_bf16 v[112:127], v[136:139], v[192:195], v[112:127]\n" \
  "ds_read_b64_tr_b16 v[128:129], v246 offset:15424\n" \
  "ds_read_b64_tr_b16 v[130:131], v247 offset:15424\n" \
  "s_waitcnt vmcnt(9) lgkmcnt(6)\n" \
  "v_mfma_f32_32x32x16_bf16 v[80:95], v[140:143], v[196:199], v[80:95]\n" \
  "ds_read_b64_tr_b16 v[132:133], v246 offset:16896\n" \
  "ds_read_b64_tr_b16 v[134:135], v247 offset:16896\n" \
  "buffer_load_dwordx4 v[192:195], v251, s[12:15], s48 offen offset:3072 sc1\n" \
  "v_mfma_f32_32x32x16_bf16 v[96:111], v[144:147], v[196:199], v[96:111]\n" \
  "ds_read_b64_tr_b16 v[136:137], v246 offset:16928\n" \
  "ds_read_b64_tr_b16 v[138:139], v247 offset:16928\n" \
  "v_mov_b32 v204, v66\n" \
  "v_mov_b32 v205, v67\n" \
  "v_mfma_f32_32x32x16_bf16 v[112:127], v[148:151], v[196:199], v[112:127]\n" \
  "ds_read_b64_tr_b16 v[140:141], v246 offset:16960\n" \
  "ds_read_b64_tr_b16 v[142:143], v247 offset:16960\n" \
  "s_waitcnt vmcnt(9) lgkmcnt(6)\n" \
  "v_mfma_f32_32x32x16_bf16 v[80:95], v[152:155], v[200:203], v[80:95]\n" \
  "ds_read_b64_tr_b16 v[144:145], v246 offset:18432\n" \
  "ds_read_b64_tr_b16 v[146:147], v247 offset:18432\n" \
  "s_xor_b32 s48, s64, 20480\n" \
  "buffer_load_dwordx4 v[196:199], v251, s[12:15], s48 offen offset:0 sc1\n" \
  "v_mfma_f32_32x32x16_bf16 v[96:111], v[156:159], v[200:203], v[96:111]\n" \
  "ds_read_b64_tr_b16 v[148:149], v246 offset:18464\n" \
  "ds_read_b64_tr_b16 v[150:151], v247 offset:18464\n" \
  "v_permlane32_swap_b32 v66, v204\n" \
  "v_permlane32_swap_b32 v67, v205\n" \
  "v_mfma_f32_32x32x16_bf16 v[112:127], v[128:131], v[200:203], v[112:127]\n" \
  "ds_read_b64_tr_b16 v[152:153], v246 offset:18496\n" \
  "ds_read_b64_tr_b16 v[154:155], v247 offset:18496\n" \
  "s_waitcnt vmcnt(9) lgkmcnt(6)\n" \
  "v_mfma_f32_32x32x16_bf16 v[80:95], v[132:135], v[160:163], v[80:95]\n" \
  "ds_read_b64_tr_b16 v[156:157], v246 offset:19968\n" \
  "ds_read_b64_tr_b16 v[158:159], v247 offset:19968\n" \
  "buffer_load_dwordx4 v[200:203], v251, s[12:15], s48 offen offset:1024 sc1\n" \
  "v_mfma_f32_32x32x16_bf16 v[96:111], v[136:139], v[160:163], v[96:111]\n" \
  "ds_read_b64_tr_b16 v[128:129], v246 offset:20000\n" \
  "ds_read_b64_tr_b16 v[130:131], v247 offset:20000\n" \
  "v_add_f32 v66, v66, v204\n" \
  "v_add_f32 v67, v67, v205\n" \
  "v_mfma_f32_32x32x16_bf16 v[112:127], v[140:143], v[160:163], v[112:127]\n" \
  "ds_read_b64_tr_b16 v[132:133], v246 offset:20032\n" \
  "ds_read_b64_tr_b16 v[134:135], v247 offset:20032\n" \
  "s_waitcnt vmcnt(9) lgkmcnt(6)\n" \
  "v_mfma_f32_32x32x16_bf16 v[80:95], v[144:147], v[164:167], v[80:95]\n" \
  "ds_read_b64_tr_b16 v[136:137], v246 offset:21504\n" \
  "ds_read_b64_tr_b16 v[138:139], v247 offset:21504\n" \
  "buffer_load_dwordx4 v[160:163], v251, s[12:15], s48 offen offset:2048 sc1\n" \
  "v_mfma_f32_32x32x16_bf16 v[96:111], v[148:151], v[164:167], v[96:111]\n" \
  "ds_read_b64_tr_b16 v[140:141], v246 offset:21536\n" \
  "ds_read_b64_tr_b16 v[142:143], v247 offset:21536\n" \
  "v_mul_f32 v66, 0xbb000000, v66\n" \
  "v_mul_f32 v67, 0x3b000000, v67\n" \
  "v_mfma_f32_32x32x16_bf16 v[112:127], v[152:155], v[164:167], v[112:127]\n" \
  "ds_read_b64_tr_b16 v[144:145], v246 offset:21568\n" \
  "ds_read_b64_tr_b16 v[146:147], v247 offset:21568\n" \
  "s_waitcnt vmcnt(9) lgkmcnt(6)\n" \
  "v_mfma_f32_32x32x16_bf16 v[80:95], v[156:159], v[168:171], v[80:95]\n" \
  "ds_read_b64_tr_b16 v[148:149], v246 offset:23040\n" \
  "ds_read_b64_tr_b16 v[150:151], v247 offset:23040\n" \
  "buffer_load_dwordx4 v[164:167], v251, s[12:15], s48 offen offset:3072 sc1\n" \
  "v_mfma_f32_32x32x16_bf16 v[96:111], v[128:131], v[168:171], v[96:111]\n" \
  "ds_read_b64_tr_b16 v[152:153], v246 offset:23072\n" \
  "ds_read_b64_tr_b16 v[154:155], v247 offset:23072\n" \
  "v_fma_f32 v67, -v66, v66, v67\n" \
  "v_max_f32 v67, 0, v67\n" \
  "v_mfma_f32_32x32x16_bf16 v[112:127], v[132:135], v[168:171], v[112:127]\n" \
  "ds_read_b64_tr_b16 v[156:157], v246 offset:23104\n" \
  "ds_read_b64_tr_b16 v[158:159], v247 offset:23104\n" \
  "s_waitcnt vmcnt(9) lgkmcnt(6)\n" \
  "v_mfma_f32_32x32x16_bf16 v[80:95], v[136:139], v[172:175], v[80:95]\n" \
  "ds_read_b64_tr_b16 v[128:129], v246 offset:24576\n" \
  "ds_read_b64_tr_b16 v[130:131], v247 offset:24576\n" \
  "s_xor_b32 s48, s64, 24576\n" \
  "buffer_load_dwordx4 v[168:171], v251, s[12:15], s48 offen offset:0 sc1\n" \
  "v_mfma_f32_32x32x16_bf16 v[96:111], v[140:143], v[172:175], v[96:111]\n" \
  "ds_read_b64_tr_b16 v[132:133], v246 offset:24608\n" \
  "ds_read_b64_tr_b16 v[134:135], v247 offset:24608\n" \
  "v_add_f32 v67, 0x3727c5ac, v67\n" \
  "v_cvt_pk_bf16_f32 v204, v66, v66\n" \
  "v_mfma_f32_32x32x16_bf16 v[112:127], v[144:147], v[172:175], v[112:127]\n" \
  "ds_read_b64_tr_b16 v[136:137], v246 offset:24640\n" \
  "ds_read_b64_tr_b16 v[138:139], v247 offset:24640\n" \
  "s_waitcnt vmcnt(9) lgkmcnt(6)\n" \
  "v_mfma_f32_32x32x16_bf16 v[80:95], v[148:151], v[176:179], v[80:95]\n" \
  "ds_read_b64_tr_b16 v[140:141], v246 offset:26112\n" \
  "ds_read_b64_tr_b16 v[142:143], v247 offset:26112\n" \
  "buffer_load_dwordx4 v[172:175], v251, s[12:15], s48 offen offset:1024 sc1\n" \
  "v_mfma_f32_32x32x16_bf16 v[96:111], v[152:155], v[176:179], v[96:111]\n" \
  "ds_read_b64_tr_b16 v[144:145], v246 offset:26144\n" \
  "ds_read_b64_tr_b16 v[146:147], v247 offset:26144\n" \
  "v_and_b32 v204, 0xffff0000, v204\n" \
  "v_sub_f32 v204, v66, v204\n" \
  "v_mfma_f32_32x32x16_bf16 v[112:127], v[156:159], v[176:179], v[112:127]\n" \
  "ds_read_b64_tr_b16 v[148:149], v246 offset:26176\n" \
  "ds_read_b64_tr_b16 v[150:151], v247 offset:26176\n" \
  "s_waitcnt vmcnt(9) lgkmcnt(6)\n" \
  "v_mfma_f32_32x32x16_bf16 v[80:95], v[128:131], v[180:183], v[80:95]\n" \
  "ds_read_b64_tr_b16 v[152:153], v246 offset:27648\n" \
  "ds_read_b64_tr_b16 v[154:155], v247 offset:27648\n" \
  "buffer_load_dwordx4 v[176:179], v251, s[12:15], s48 offen offset:2048 sc1\n" \
  "v_mfma_f32_32x32x16_bf16 v[96:111], v[132:135], v[180:183], v[96:111]\n" \
  "ds_read_b64_tr_b16 v[156:157], v246 offset:27680\n" \
  "ds_read_b64_tr_b16 v[158:159], v247 offset:27680\n" \
  "v_cvt_pk_bf16_f32 v78, v66, v204\n" \
  "v_cvt_pk_bf16_f32 v79, v66, v204\n" \
  "v_mfma_f32_32x32x16_bf16 v[112:127], v[136:139], v[180:183], v[112:127]\n" \
  "ds_read_b64_tr_b16 v[128:129], v246 offset:27712\n" \
  "ds_read_b64_tr_b16 v[130:131], v247 offset:27712\n" \
  "s_waitcnt vmcnt(9) lgkmcnt(6)\n" \
  "v_mfma_f32_32x32x16_bf16 v[80:95], v[140:143], v[184:187], v[80:95]\n" \
  "ds_read_b64_tr_b16 v[132:133], v246 offset:29184\n" \
  "ds_read_b64_tr_b16 v[134:135], v247 offset:29184\n" \
  "buffer_load_dwordx4 v[180:183], v251, s[12:15], s48 offen offset:3072 sc1\n" \
  "v_mfma_f32_32x32x16_bf16 v[96:111], v[144:147], v[184:187], v[96:111]\n" \
  "ds_read_b64_tr_b16 v[136:137], v246 offset:29216\n" \
  "ds_read_b64_tr_b16 v[138:139], v247 offset:29216\n" \
  "v_rsq_f32 v68, v67\n" \
  "s_nop 0\n" \
  "v_mul_f32 v68, 0xbfb8aa3b, v68\n" \
  "v_mfma_f32_32x32x16_bf16 v[112:127], v[148:151], v[184:187], v[112:127]\n" \
  "ds_read_b64_tr_b16 v[140:141], v246 offset:29248\n" \
  "ds_read_b64_tr_b16 v[142:143], v247 offset:29248\n" \
  "s_waitcnt vmcnt(9) lgkmcnt(6)\n" \
  "v_mfma_f32_32x32x16_bf16 v[80:95], v[152:155], v[188:191], v[80:95]\n" \
  "ds_read_b64_tr_b16 v[144:145], v246 offset:30720\n" \
  "ds_read_b64_tr_b16 v[146:147], v247 offset:30720\n" \
  "s_xor_b32 s48, s64, 28672\n" \
  "buffer_load_dwordx4 v[184:187], v251, s[12:15], s48 offen offset:0 sc1\n" \
  "v_mfma_f32_32x32x16_bf16 v[96:111], v[156:159], v[188:191], v[96:111]\n" \
  "ds_read_b64_tr_b16 v[148:149], v246 offset:30752\n" \
  "ds_read_b64_tr_b16 v[150:151], v247 offset:30752\n" \
  "v_mfma_f32_32x32x16_bf16 v[112:127], v[128:131], v[188:191], v[112:127]\n" \
  "ds_read_b64_tr_b16 v[152:153], v246 offset:30784\n" \
  "ds_read_b64_tr_b16 v[154:155], v247 offset:30784\n" \
  "s_waitcnt vmcnt(9) lgkmcnt(6)\n" \
  "v_mfma_f32_32x32x16_bf16 v[80:95], v[132:135], v[192:195], v[80:95]\n" \
  "ds_read_b64_tr_b16 v[156:157], v246 offset:32256\n" \
  "ds_read_b64_tr_b16 v[158:159], v247 offset:32256\n" \
  "buffer_load_dwordx4 v[188:191], v251, s[12:15], s48 offen offset:1024 sc1\n" \
  "v_mfma_f32_32x32x16_bf16 v[96:111], v[136:139], v[192:195], v[96:111]\n" \
  "ds_read_b64_tr_b16 v[128:129], v246 offset:32288\n" \
  "ds_read_b64_tr_b16 v[130:131], v247 offset:32288\n" \
  "v_mfma_f32_32x32x16_bf16 v[112:127], v[140:143], v[192:195], v[112:127]\n" \
  "ds_read_b64_tr_b16 v[132:133], v246 offset:32320\n" \
  "ds_read_b64_tr_b16 v[134:135], v247 offset:32320\n" \
  "s_waitcnt vmcnt(9) lgkmcnt(6)\n" \
  "v_mfma_f32_32x32x16_bf16 v[80:95], v[144:147], v[196:199], v[80:95]\n" \
  "ds_read_b64_tr_b16 v[136:137], v246 offset:33792\n" \
  "ds_read_b64_tr_b16 v[138:139], v247 offset:33792\n" \
  "buffer_load_dwordx4 v[192:195], v251, s[12:15], s48 offen offset:2048 sc1\n" \
  "v_mfma_f32_32x32x16_bf16 v[96:111], v[148:151], v[196:199], v[96:111]\n" \
  "ds_read_b64_tr_b16 v[140:141], v246 offset:33824\n" \
  "ds_read_b64_tr_b16 v[142:143], v247 offset:33824\n" \
  "v_mfma_f32_32x32x16_bf16 v[112:127], v[152:155], v[196:199], v[112:127]\n" \
  "ds_read_b64_tr_b16 v[144:145], v246 offset:33856\n" \
  "ds_read_b64_tr_b16 v[146:147], v247 offset:33856\n" \
  "s_waitcnt vmcnt(9) lgkmcnt(6)\n" \
  "v_mfma_f32_32x32x16_bf16 v[80:95], v[156:159], v[200:203], v[80:95]\n" \
  "ds_read_b64_tr_b16 v[148:149], v246 offset:35328\n" \
  "ds_read_b64_tr_b16 v[150:151], v247 offset:35328\n" \
  "buffer_load_dwordx4 v[196:199], v251, s[12:15], s48 offen offset:3072 sc1\n" \
  "v_mfma_f32_32x32x16_bf16 v[96:111], v[128:131], v[200:203], v[96:111]\n" \
  "ds_read_b64_tr_b16 v[152:153], v246 offset:35360\n" \
  "ds_read_b64_tr_b16 v[154:155], v247 offset:35360\n" \
  "v_mfma_f32_32x32x16_bf16 v[112:127], v[132:135], v[200:203], v[112:127]\n" \
  "ds_read_b64_tr_b16 v[156:157], v246 offset:35392\n" \
  "ds_read_b64_tr_b16 v[158:159], v247 offset:35392\n" \
  "s_waitcnt vmcnt(9) lgkmcnt(6)\n" \
  "v_mfma_f32_32x32x16_bf16 v[80:95], v[136:139], v[160:163], v[80:95]\n" \
  "ds_read_b64_tr_b16 v[128:129], v246 offset:36864\n" \
  "ds_read_b64_tr_b16 v[130:131], v247 offset:36864\n" \
  "s_add_u32 s48, s74, 507904\n" \
  "buffer_load_dwordx4 v[200:203], v254, s[24:27], s48 offen\n" \
  "v_mfma_f32_32x32x16_bf16 v[96:111], v[140:143], v[160:163], v[96:111]\n" \
  "ds_read_b64_tr_b16 v[132:133], v246 offset:36896\n" \
  "ds_read_b64_tr_b16 v[134:135], v247 offset:36896\n" \
  "v_mfma_f32_32x32x16_bf16 v[112:127], v[144:147], v[160:163], v[112:127]\n" \
  "ds_read_b64_tr_b16 v[136:137], v246 offset:36928\n" \
  "ds_read_b64_tr_b16 v[138:139], v247 offset:36928\n" \
  "s_waitcnt vmcnt(9) lgkmcnt(6)\n" \
  "v_mfma_f32_32x32x16_bf16 v[80:95], v[148:151], v[164:167], v[80:95]\n" \
  "ds_read_b64_tr_b16 v[140:141], v246 offset:38400\n" \
  "ds_read_b64_tr_b16 v[142:143], v247 offset:38400\n" \
  "s_add_u32 s48, s74, 524288\n" \
  "buffer_load_dwordx4 v[160:163], v254, s[24:27], s48 offen\n" \
  "v_mfma_f32_32x32x16_bf16 v[96:111], v[152:155], v[164:167], v[96:111]\n" \
  "ds_read_b64_tr_b16 v[144:145], v246 offset:38432\n" \
  "ds_read_b64_tr_b16 v[146:147], v247 offset:38432\n" \
  "v_mfma_f32_32x32x16_bf16 v[112:127], v[156:159], v[164:167], v[112:127]\n" \
  "ds_read_b64_tr_b16 v[148:149], v246 offset:38464\n" \
  "ds_read_b64_tr_b16 v[150:151], v247 offset:38464\n" \
  "s_waitcnt vmcnt(9) lgkmcnt(6)\n" \
  "v_mfma_f32_32x32x16_bf16 v[80:95], v[128:131], v[168:171], v[80:95]\n" \
  "ds_read_b64_tr_b16 v[152:153], v246 offset:39936\n" \
  "ds_read_b64_tr_b16 v[154:155], v247 offset:39936\n" \
  "s_add_u32 s48, s74, 589824\n" \
  "buffer_load_dwordx4 v[164:167], v254, s[24:27], s48 offen\n" \
  "v_mfma_f32_32x32x16_bf16 v[96:111], v[132:135], v[168:171], v[96:111]\n" \
  "ds_read_b64_tr_b16 v[156:157], v246 offset:39968\n" \
  "ds_read_b64_tr_b16 v[158:159], v247 offset:39968\n" \
  "v_mfma_f32_32x32x16_bf16 v[112:127], v[136:139], v[168:171], v[112:127]\n" \
  "ds_read_b64_tr_b16 v[128:129], v246 offset:40000\n" \
  "ds_read_b64_tr_b16 v[130:131], v247 offset:40000\n" \
    \
  "v_mfma_f32_32x32x8_bf16 v[80:95], v[72:73], v[78:79], v[80:95]\n" \
  "v_mfma_f32_32x32x8_bf16 v[96:111], v[74:75], v[78:79], v[96:111]\n" \
  "v_mfma_f32_32x32x8_bf16 v[112:127], v[76:77], v[78:79], v[112:127]\n" \
  "s_waitcnt vmcnt(9) lgkmcnt(6)\n" \
  "v_mfma_f32_32x32x16_bf16 v[80:95], v[140:143], v[172:175], v[80:95]\n" \
  "ds_read_b64_tr_b16 v[132:133], v246 offset:41472\n" \
  "ds_read_b64_tr_b16 v[134:135], v247 offset:41472\n" \
  "s_add_u32 s48, s74, 606208\n" \
  "buffer_load_dwordx4 v[168:171], v254, s[24:27], s48 offen\n" \
  "v_mfma_f32_32x32x16_bf16 v[96:111], v[144:147], v[172:175], v[96:111]\n" \
  "ds_read_b64_tr_b16 v[136:137], v246 offset:41504\n" \
  "ds_read_b64_tr_b16 v[138:139], v247 offset:41504\n" \
  "v_mfma_f32_32x32x16_bf16 v[112:127], v[148:151], v[172:175], v[112:127]\n" \
  "ds_read_b64_tr_b16 v[140:141], v246 offset:41536\n" \
  "ds_read_b64_tr_b16 v[142:143], v247 offset:41536\n" \
  "s_waitcnt vmcnt(9) lgkmcnt(6)\n" \
  "v_mfma_f32_32x32x16_bf16 v[80:95], v[152:155], v[176:179], v[80:95]\n" \
  "ds_read_b64_tr_b16 v[144:145], v246 offset:43008\n" \
  "ds_read_b64_tr_b16 v[146:147], v247 offset:43008\n" \
  "s_add_u32 s48, s74, 622592\n" \
  "buffer_load_dwordx4 v[172:175], v254, s[24:27], s48 offen\n" \
  "v_mfma_f32_32x32x16_bf16 v[96:111], v[156:159], v[176:179], v[96:111]\n" \
  "ds_read_b64_tr_b16 v[148:149], v246 offset:43040\n" \
  "ds_read_b64_tr_b16 v[150:151], v247 offset:43040\n" \
  "v_mfma_f32_32x32x16_bf16 v[112:127], v[128:131], v[176:179], v[112:127]\n" \
  "ds_read_b64_tr_b16 v[152:153], v246 offset:43072\n" \
  "ds_read_b64_tr_b16 v[154:155], v247 offset:43072\n" \
  "s_waitcnt vmcnt(9) lgkmcnt(6)\n" \
  "v_mfma_f32_32x32x16_bf16 v[80:95], v[132:135], v[180:183], v[80:95]\n" \
  "ds_read_b64_tr_b16 v[156:157], v246 offset:44544\n" \
  "ds_read_b64_tr_b16 v[158:159], v247 offset:44544\n" \
  "s_add_u32 s48, s62, 491520\n" \
  "buffer_load_dwordx4 v[176:179], v254, s[24:27], s48 offen\n" \
  "v_mfma_f32_32x32x16_bf16 v[96:111], v[136:139], v[180:183], v[96:111]\n" \
  "ds_read_b64_tr_b16 v[128:129], v246 offset:44576\n" \
  "ds_read_b64_tr_b16 v[130:131], v247 offset:44576\n" \
  "v_mfma_f32_32x32x16_bf16 v[112:127], v[140:143], v[180:183], v[112:127]\n" \
  "ds_read_b64_tr_b16 v[132:133], v246 offset:44608\n" \
  "ds_read_b64_tr_b16 v[134:135], v247 offset:44608\n" \
  "s_waitcnt vmcnt(9) lgkmcnt(6)\n" \
  "v_mfma_f32_32x32x16_bf16 v[80:95], v[144:147], v[184:187], v[80:95]\n" \
  "ds_read_b64_tr_b16 v[136:137], v246 offset:46080\n" \
  "ds_read_b64_tr_b16 v[138:139], v247 offset:46080\n" \
  "s_add_u32 s48, s62, 507904\n" \
  "buffer_load_dwordx4 v[180:183], v254, s[24:27], s48 offen\n" \
  "v_mfma_f32_32x32x16_bf16 v[96:111], v[148:151], v[184:187], v[96:111]\n" \
  "ds_read_b64_tr_b16 v[140:141], v246 offset:46112\n" \
  "ds_read_b64_tr_b16 v[142:143], v247 offset:46112\n" \
  "v_mfma_f32_32x32x16_bf16 v[112:127], v[152:155], v[184:187], v[112:127]\n" \
  "ds_read_b64_tr_b16 v[144:145], v246 offset:46144\n" \
  "ds_read_b64_tr_b16 v[146:147], v247 offset:46144\n" \
  "s_waitcnt vmcnt(9) lgkmcnt(6)\n" \
  "v_mfma_f32_32x32x16_bf16 v[80:95], v[156:159], v[188:191], v[80:95]\n" \
  "ds_read_b64_tr_b16 v[148:149], v246 offset:47616\n" \
  "ds_read_b64_tr_b16 v[150:151], v247 offset:47616\n" \
  "s_add_u32 s48, s62, 524288\n" \
  "buffer_load_dwordx4 v[184:187], v254, s[24:27], s48 offen\n" \
  "v_mfma_f32_32x32x16_bf16 v[96:111], v[128:131], v[188:191], v[96:111]\n" \
  "ds_read_b64_tr_b16 v[152:153], v246 offset:47648\n" \
  "ds_read_b64_tr_b16 v[154:155], v247 offset:47648\n" \
  "v_mfma_f32_32x32x16_bf16 v[112:127], v[132:135], v[188:191], v[112:127]\n" \
  "ds_read_b64_tr_b16 v[156:157], v246 offset:47680\n" \
  "ds_read_b64_tr_b16 v[158:159], v247 offset:47680\n" \
  "s_waitcnt vmcnt(9) lgkmcnt(6)\n" \
  "v_mfma_f32_32x32x16_bf16 v[80:95], v[136:139], v[192:195], v[80:95]\n" \
  "s_add_u32 s48, s62, 589824\n" \
  "buffer_load_dwordx4 v[188:191], v254, s[24:27], s48 offen\n" \
  "v_mfma_f32_32x32x16_bf16 v[96:111], v[140:143], v[192:195], v[96:111]\n" \
  "v_mfma_f32_32x32x16_bf16 v[112:127], v[144:147], v[192:195], v[112:127]\n" \
    \
  "s_cmp_ge_u32 s36, 4\n" \
  "s_cbranch_scc1 fbk_d8\n" \
  "s_barrier\n" \
  "fbk_d8:\n" \
  "s_waitcnt vmcnt(9) lgkmcnt(0)\n" \
  "v_mfma_f32_32x32x16_bf16 v[80:95], v[148:151], v[196:199], v[80:95]\n" \
  "s_add_u32 s48, s62, 606208\n" \
  "buffer_load_dwordx4 v[192:195], v254, s[24:27], s48 offen\n" \
    \
  "s_cmp_lt_u32 s36, 4\n" \
  "s_cbranch_scc1 fbk_d7\n" \
  "s_barrier\n" \
  "fbk_d7:\n" \
  "v_mfma_f32_32x32x16_bf16 v[96:111], v[152:155], v[196:199], v[96:111]\n" \
  "v_mfma_f32_32x32x16_bf16 v[112:127], v[156:159], v[196:199], v[112:127]\n" \
  "s_cmp_lt_u32 s36, 4\n" \
  "s_cbranch_scc1 fbk_d9\n" \
    \
  "s_nop 3\n" \
  "v_pk_mul_f32 v[204:205], v[80:81], v[68:69] op_sel_hi:[1,0]\n" \
  "v_pk_mul_f32 v[206:207], v[82:83], v[68:69] op_sel_hi:[1,0]\n" \
  "v_pk_mul_f32 v[208:209], v[84:85], v[68:69] op_sel_hi:[1,0]\n" \
  "v_pk_mul_f32 v[210:211], v[86:87], v[68:69] op_sel_hi:[1,0]\n" \
  "v_exp_f32 v204, v204\n" \
  "v_exp_f32 v205, v205\n" \
  "v_exp_f32 v206, v206\n" \
  "v_exp_f32 v207, v207\n" \
  "v_exp_f32 v208, v208\n" \
  "v_exp_f32 v209, v209\n" \
  "v_exp_f32 v210, v210\n" \
  "v_exp_f32 v211, v211\n" \
  "v_pk_mul_f32 v[80:81], v[80:81], v[88:89]\n" \
  "v_pk_mul_f32 v[82:83], v[82:83], v[90:91]\n" \
  "v_pk_mul_f32 v[84:85], v[84:85], v[92:93]\n" \
  "v_pk_mul_f32 v[86:87], v[86:87], v[94:95]\n" \
  "v_pk_fma_f32 v[204:205], v[204:205], v[66:67], v[66:67] op_sel:[0,1,1] op_sel_hi:[1,1,1]\n" \
  "v_pk_fma_f32 v[206:207], v[206:207], v[66:67], v[66:67] op_sel:[0,1,1] op_sel_hi:[1,1,1]\n" \
  "v_pk_fma_f32 v[208:209], v[208:209], v[66:67], v[66:67] op_sel:[0,1,1] op_sel_hi:[1,1,1]\n" \
  "v_pk_fma_f32 v[210:211], v[210:211], v[66:67], v[66:67] op_sel:[0,1,1] op_sel_hi:[1,1,1]\n" \
  "v_rcp_f32 v204, v204\n" \
  "v_rcp_f32 v205, v205\n" \
  "v_rcp_f32 v206, v206\n" \
  "v_rcp_f32 v207, v207\n" \
  "v_rcp_f32 v208, v208\n" \
  "v_rcp_f32 v209, v209\n" \
  "v_rcp_f32 v210, v210\n" \
  "v_rcp_f32 v211, v211\n" \
  "v_pk_mul_f32 v[80:81], v[80:81], v[204:205]\n" \
  "v_pk_mul_f32 v[82:83], v[82:83], v[206:207]\n" \
  "v_pk_mul_f32 v[84:85], v[84:85], v[208:209]\n" \
  "v_pk_mul_f32 v[86:87], v[86:87], v[210:211]\n" \
  "v_cvt_pk_bf16_f32 v88, v80, v81\n" \
  "v_cvt_pk_bf16_f32 v89, v82, v83\n" \
  "v_cvt_pk_bf16_f32 v90, v84, v85\n" \
  "v_cvt_pk_bf16_f32 v91, v86, v87\n" \
  "buffer_store_dwordx4 v[88:91], v251, s[12:15], s65 offen offset:0 sc1\n" \
    \
  "v_pk_mul_f32 v[212:213], v[96:97], v[68:69] op_sel_hi:[1,0]\n" \
  "v_pk_mul_f32 v[214:215], v[98:99], v[68:69] op_sel_hi:[1,0]\n" \
  "v_pk_mul_f32 v[216:217], v[100:101], v[68:69] op_sel_hi:[1,0]\n" \
  "v_pk_mul_f32 v[218:219], v[102:103], v[68:69] op_sel_hi:[1,0]\n" \
  "v_exp_f32 v212, v212\n" \
  "v_exp_f32 v213, v213\n" \
  "v_exp_f32 v214, v214\n" \
  "v_exp_f32 v215, v215\n" \
  "v_exp_f32 v216, v216\n" \
  "v_exp_f32 v217, v217\n" \
  "v_exp_f32 v218, v218\n" \
  "v_exp_f32 v219, v219\n" \
  "v_pk_mul_f32 v[96:97], v[96:97], v[104:105]\n" \
  "v_pk_mul_f32 v[98:99], v[98:99], v[106:107]\n" \
  "v_pk_mul_f32 v[100:101], v[100:101], v[108:109]\n" \
  "v_pk_mul_f32 v[102:103], v[102:103], v[110:111]\n" \
  "v_pk_fma_f32 v[212:213], v[212:213], v[66:67], v[66:67] op_sel:[0,1,1] op_sel_hi:[1,1,1]\n" \
  "v_pk_fma_f32 v[214:215], v[214:215], v[66:67], v[66:67] op_sel:[0,1,1] op_sel_hi:[1,1,1]\n" \
  "v_pk_fma_f32 v[216:217], v[216:217], v[66:67], v[66:67] op_sel:[0,1,1] op_sel_hi:[1,1,1]\n" \
  "v_pk_fma_f32 v[218:219], v[218:219], v[66:67], v[66:67] op_sel:[0,1,1] op_sel_hi:[1,1,1]\n" \
  "v_rcp_f32 v212, v212\n" \
  "v_rcp_f32 v213, v213\n" \
  "v_rcp_f32 v214, v214\n" \
  "v_rcp_f32 v215, v215\n" \
  "v_rcp_f32 v216, v216\n" \
  "v_rcp_f32 v217, v217\n" \
  "v_rcp_f32 v218, v218\n" \
  "v_rcp_f32 v219, v219\n" \
  "v_pk_mul_f32 v[96:97], v[96:97], v[212:213]\n" \
  "v_pk_mul_f32 v[98:99], v[98:99], v[214:215]\n" \
  "v_pk_mul_f32 v[100:101], v[100:101], v[216:217]\n" \
  "v_pk_mul_f32 v[102:103], v[102:103], v[218:219]\n" \
  "v_cvt_pk_bf16_f32 v104, v96, v97\n" \
  "v_cvt_pk_bf16_f32 v105, v98, v99\n" \
  "v_cvt_pk_bf16_f32 v106, v100, v101\n" \
  "v_cvt_pk_bf16_f32 v107, v102, v103\n" \
  "buffer_store_dwordx4 v[104:107], v251, s[12:15], s65 offen offset:1024 sc1\n" \
  "s_cmp_eq_u32 s38, 28\n" \
  "s_cbranch_scc1 fbk_e0\n" \
    \
  "v_pk_mul_f32 v[220:221], v[112:113], v[68:69] op_sel_hi:[1,0]\n" \
  "v_pk_mul_f32 v[222:223], v[114:115], v[68:69] op_sel_hi:[1,0]\n" \
  "v_pk_mul_f32 v[224:225], v[116:117], v[68:69] op_sel_hi:[1,0]\n" \
  "v_pk_mul_f32 v[226:227], v[118:119], v[68:69] op_sel_hi:[1,0]\n" \
  "v_exp_f32 v220, v220\n" \
  "v_exp_f32 v221, v221\n" \
  "v_exp_f32 v222, v222\n" \
  "v_exp_f32 v223, v223\n" \
  "v_exp_f32 v224, v224\n" \
  "v_exp_f32 v225, v225\n" \
  "v_exp_f32 v226, v226\n" \
  "v_exp_f32 v227, v227\n" \
  "v_pk_mul_f32 v[112:113], v[112:113], v[120:121]\n" \
  "v_pk_mul_f32 v[114:115], v[114:115], v[122:123]\n" \
  "v_pk_mul_f32 v[116:117], v[116:117], v[124:125]\n" \
  "v_pk_mul_f32 v[118:119], v[118:119], v[126:127]\n" \
  "v_pk_fma_f32 v[220:221], v[220:221], v[66:67], v[66:67] op_sel:[0,1,1] op_sel_hi:[1,1,1]\n" \
  "v_pk_fma_f32 v[222:223], v[222:223], v[66:67], v[66:67] op_sel:[0,1,1] op_sel_hi:[1,1,1]\n" \
  "v_pk_fma_f32 v[224:225], v[224:225], v[66:67], v[66:67] op_sel:[0,1,1] op_sel_hi:[1,1,1]\n" \
  "v_pk_fma_f32 v[226:227], v[226:227], v[66:67], v[66:67] op_sel:[0,1,1] op_sel_hi:[1,1,1]\n" \
  "v_rcp_f32 v220, v220\n" \
  "v_rcp_f32 v221, v221\n" \
  "v_rcp_f32 v222, v222\n" \
  "v_rcp_f32 v223, v223\n" \
  "v_rcp_f32 v224, v224\n" \
  "v_rcp_f32 v225, v225\n" \
  "v_rcp_f32 v226, v226\n" \
  "v_rcp_f32 v227, v227\n" \
  "v_pk_mul_f32 v[112:113], v[112:113], v[220:221]\n" \
  "v_pk_mul_f32 v[114:115], v[114:115], v[222:223]\n" \
  "v_pk_mul_f32 v[116:117], v[116:117], v[224:225]\n" \
  "v_pk_mul_f32 v[118:119], v[118:119], v[226:227]\n" \
  "v_cvt_pk_bf16_f32 v120, v112, v113\n" \
  "v_cvt_pk_bf16_f32 v121, v114, v115\n" \
  "v_cvt_pk_bf16_f32 v122, v116, v117\n" \
  "v_cvt_pk_bf16_f32 v123, v118, v119\n" \
  "buffer_store_dwordx4 v[120:123], v251, s[12:15], s65 offen offset:2048 sc1\n" \
  "s_branch fbk_e0\n" \
  "fbk_d9:\n" \
  "s_setprio 0\n" \
  "s_add_u32 s48, s62, 622592\n" \
  "buffer_load_dwordx4 v[196:199], v254, s[24:27], s48 offen\n" \
    \
  "s_nop 0\n" \
  "v_mul_f32 v204, v68, v80\n" \
  "v_mul_f32 v205, v68, v81\n" \
  "v_mul_f32 v206, v68, v82\n" \
  "v_mul_f32 v207, v68, v83\n" \
  "v_mul_f32 v208, v68, v84\n" \
  "v_mul_f32 v209, v68, v85\n" \
  "v_mul_f32 v210, v68, v86\n" \
  "v_mul_f32 v211, v68, v87\n" \
  "v_exp_f32 v204, v204\n" \
  "v_exp_f32 v205, v205\n" \
  "v_exp_f32 v206, v206\n" \
  "v_exp_f32 v207, v207\n" \
  "v_exp_f32 v208, v208\n" \
  "v_exp_f32 v209, v209\n" \
  "v_exp_f32 v210, v210\n" \
  "v_exp_f32 v211, v211\n" \
  "v_mul_f32 v80, v80, v88\n" \
  "v_mul_f32 v81, v81, v89\n" \
  "v_mul_f32 v82, v82, v90\n" \
  "v_mul_f32 v83, v83, v91\n" \
  "v_mul_f32 v84, v84, v92\n" \
  "v_mul_f32 v85, v85, v93\n" \
  "v_mul_f32 v86, v86, v94\n" \
  "v_mul_f32 v87, v87, v95\n" \
  "v_fma_f32 v204, v204, v67, v67\n" \
  "v_fma_f32 v205, v205, v67, v67\n" \
  "v_fma_f32 v206, v206, v67, v67\n" \
  "v_fma_f32 v207, v207, v67, v67\n" \
  "v_fma_f32 v208, v208, v67, v67\n" \
  "v_fma_f32 v209, v209, v67, v67\n" \
  "v_fma_f32 v210, v210, v67, v67\n" \
  "v_fma_f32 v211, v211, v67, v67\n" \
  "v_rcp_f32 v204, v204\n" \
  "v_rcp_f32 v205, v205\n" \
  "v_rcp_f32 v206, v206\n" \
  "v_rcp_f32 v207, v207\n" \
  "v_rcp_f32 v208, v208\n" \
  "v_rcp_f32 v209, v209\n" \
  "v_rcp_f32 v210, v210\n" \
  "v_rcp_f32 v211, v211\n" \
  "v_mul_f32 v80, v80, v204\n" \
  "v_mul_f32 v81, v81, v205\n" \
  "v_mul_f32 v82, v82, v206\n" \
  "v_mul_f32 v83, v83, v207\n" \
  "v_mul_f32 v84, v84, v208\n" \
  "v_mul_f32 v85, v85, v209\n" \
  "v_mul_f32 v86, v86, v210\n" \
  "v_mul_f32 v87, v87, v211\n" \
  "v_cvt_pk_bf16_f32 v88, v80, v81\n" \
  "v_cvt_pk_bf16_f32 v89, v82, v83\n" \
  "v_cvt_pk_bf16_f32 v90, v84, v85\n" \
  "v_cvt_pk_bf16_f32 v91, v86, v87\n" \
  "buffer_store_dwordx4 v[88:91], v251, s[12:15], s65 offen offset:0 sc1\n" \
    \
  "v_mul_f32 v212, v68, v96\n" \
  "v_mul_f32 v213, v68, v97\n" \
  "v_mul_f32 v214, v68, v98\n" \
  "v_mul_f32 v215, v68, v99\n" \
  "v_mul_f32 v216, v68, v100\n" \
  "v_mul_f32 v217, v68, v101\n" \
  "v_mul_f32 v218, v68, v102\n" \
  "v_mul_f32 v219, v68, v103\n" \
  "v_exp_f32 v212, v212\n" \
  "v_exp_f32 v213, v213\n" \
  "v_exp_f32 v214, v214\n" \
  "v_exp_f32 v215, v215\n" \
  "v_exp_f32 v216, v216\n" \
  "v_exp_f32 v217, v217\n" \
  "v_exp_f32 v218, v218\n" \
  "v_exp_f32 v219, v219\n" \
  "v_mul_f32 v96, v96, v104\n" \
  "v_mul_f32 v97, v97, v105\n" \
  "v_mul_f32 v98, v98, v106\n" \
  "v_mul_f32 v99, v99, v107\n" \
  "v_mul_f32 v100, v100, v108\n" \
  "v_mul_f32 v101, v101, v109\n" \
  "v_mul_f32 v102, v102, v110\n" \
  "v_mul_f32 v103, v103, v111\n" \
  "v_fma_f32 v212, v212, v67, v67\n" \
  "v_fma_f32 v213, v213, v67, v67\n" \
  "v_fma_f32 v214, v214, v67, v67\n" \
  "v_fma_f32 v215, v215, v67, v67\n" \
  "v_fma_f32 v216, v216, v67, v67\n" \
  "v_fma_f32 v217, v217, v67, v67\n" \
  "v_fma_f32 v218, v218, v67, v67\n" \
  "v_fma_f32 v219, v219, v67, v67\n" \
  "v_rcp_f32 v212, v212\n" \
  "v_rcp_f32 v213, v213\n" \
  "v_rcp_f32 v214, v214\n" \
  "v_rcp_f32 v215, v215\n" \
  "v_rcp_f32 v216, v216\n" \
  "v_rcp_f32 v217, v217\n" \
  "v_rcp_f32 v218, v218\n" \
  "v_rcp_f32 v219, v219\n" \
  "v_mul_f32 v96, v96, v212\n" \
  "v_mul_f32 v97, v97, v213\n" \
  "v_mul_f32 v98, v98, v214\n" \
  "v_mul_f32 v99, v99, v215\n" \
  "v_mul_f32 v100, v100, v216\n" \
  "v_mul_f32 v101, v101, v217\n" \
  "v_mul_f32 v102, v102, v218\n" \
  "v_mul_f32 v103, v103, v219\n" \
  "v_cvt_pk_bf16_f32 v104, v96, v97\n" \
  "v_cvt_pk_bf16_f32 v105, v98, v99\n" \
  "v_cvt_pk_bf16_f32 v106, v100, v101\n" \
  "v_cvt_pk_bf16_f32 v107, v102, v103\n" \
  "buffer_store_dwordx4 v[104:107], v251, s[12:15], s65 offen offset:1024 sc1\n" \
  "s_cmp_eq_u32 s38, 28\n" \
  "s_cbranch_scc1 fbk_d10\n" \
    \
  "v_mul_f32 v220, v68, v112\n" \
  "v_mul_f32 v221, v68, v113\n" \
  "v_mul_f32 v222, v68, v114\n" \
  "v_mul_f32 v223, v68, v115\n" \
  "v_mul_f32 v224, v68, v116\n" \
  "v_mul_f32 v225, v68, v117\n" \
  "v_mul_f32 v226, v68, v118\n" \
  "v_mul_f32 v227, v68, v119\n" \
  "v_exp_f32 v220, v220\n" \
  "v_exp_f32 v221, v221\n" \
  "v_exp_f32 v222, v222\n" \
  "v_exp_f32 v223, v223\n" \
  "v_exp_f32 v224, v224\n" \
  "v_exp_f32 v225, v225\n" \
  "v_exp_f32 v226, v226\n" \
  "v_exp_f32 v227, v227\n" \
  "v_mul_f32 v112, v112, v120\n" \
  "v_mul_f32 v113, v113, v121\n" \
  "v_mul_f32 v114, v114, v122\n" \
  "v_mul_f32 v115, v115, v123\n" \
  "v_mul_f32 v116, v116, v124\n" \
  "v_mul_f32 v117, v117, v125\n" \
  "v_mul_f32 v118, v118, v126\n" \
  "v_mul_f32 v119, v119, v127\n" \
  "v_fma_f32 v220, v220, v67, v67\n" \
  "v_fma_f32 v221, v221, v67, v67\n" \
  "v_fma_f32 v222, v222, v67, v67\n" \
  "v_fma_f32 v223, v223, v67, v67\n" \
  "v_fma_f32 v224, v224, v67, v67\n" \
  "v_fma_f32 v225, v225, v67, v67\n" \
  "v_fma_f32 v226, v226, v67, v67\n" \
  "v_fma_f32 v227, v227, v67, v67\n" \
  "v_rcp_f32 v220, v220\n" \
  "v_rcp_f32 v221, v221\n" \
  "v_rcp_f32 v222, v222\n" \
  "v_rcp_f32 v223, v223\n" \
  "v_rcp_f32 v224, v224\n" \
  "v_rcp_f32 v225, v225\n" \
  "v_rcp_f32 v226, v226\n" \
  "v_rcp_f32 v227, v227\n" \
  "v_mul_f32 v112, v112, v220\n" \
  "v_mul_f32 v113, v113, v221\n" \
  "v_mul_f32 v114, v114, v222\n" \
  "v_mul_f32 v115, v115, v223\n" \
  "v_mul_f32 v116, v116, v224\n" \
  "v_mul_f32 v117, v117, v225\n" \
  "v_mul_f32 v118, v118, v226\n" \
  "v_mul_f32 v119, v119, v227\n" \
  "v_cvt_pk_bf16_f32 v120, v112, v113\n" \
  "v_cvt_pk_bf16_f32 v121, v114, v115\n" \
  "v_cvt_pk_bf16_f32 v122, v116, v117\n" \
  "v_cvt_pk_bf16_f32 v123, v118, v119\n" \
  "buffer_store_dwordx4 v[120:123], v251, s[12:15], s65 offen offset:2048 sc1\n" \
  "fbk_d10:\n" \
  "s_nop 1\n" \
    \
  "s_and_b32 s47, s63, 2048\n" \
  "v_add_u32 v24, s47, v244\n" \
  "v_xor_b32 v27, 64, v24\n" \
  "v_add_u32 v25, 65536, v24\n" \
  "v_add_u32 v28, 65536, v27\n" \
  "v_add_u32 v26, 131072, v24\n" \
  "v_add_u32 v29, 131072, v27\n" \
  "ds_read_b64_tr_b16 v[0:1], v25 offset:32768\n" \
  "ds_read_b64_tr_b16 v[2:3], v25 offset:33792\n" \
  "ds_read_b64_tr_b16 v[4:5], v28 offset:32768\n" \
  "ds_read_b64_tr_b16 v[6:7], v28 offset:33792\n" \
  "ds_read_b64_tr_b16 v[8:9], v25 offset:36864\n" \
  "ds_read_b64_tr_b16 v[10:11], v25 offset:37888\n" \
  "ds_read_b64_tr_b16 v[12:13], v28 offset:36864\n" \
  "ds_read_b64_tr_b16 v[14:15], v28 offset:37888\n" \
  "ds_read_b64_tr_b16 v[16:17], v25 offset:40960\n" \
  "ds_read_b64_tr_b16 v[18:19], v25 offset:41984\n" \
  "ds_read_b64_tr_b16 v[20:21], v28 offset:40960\n" \
  "ds_read_b64_tr_b16 v[22:23], v28 offset:41984\n" \
  "v_lshlrev_b32 v30, 7, v250\n" \
  "s_mov_b32 s67, 0xffffff\n" \
  "s_cmp_eq_u32 s38, 28\n" \
  "s_cselect_b32 s67, 0xffff, s67\n" \
  "v_lshrrev_b32 v204, 3, v250\n" \
  "v_and_b32 v205, 7, v250\n" \
  "v_lshl_add_u32 v206, s36, 3, v204\n" \
  "v_mul_u32_u24 v64, 2112, v205\n" \
  "v_lshl_add_u32 v64, v206, 4, v64\n" \
  "v_add_u32 v64, 110592, v64\n" \
  "v_lshlrev_b32 v205, 4, v205\n" \
  "v_lshl_or_b32 v65, v204, 10, v205\n" \
  "v_add_u32 v204, s63, v251\n" \
  "v_add_u32 v205, 65536, v204\n" \
    \
  "s_and_b32 s48, s36, 1\n" \
  "s_lshl2_add_u32 s47, s48, 8\n" \
  "v_add_u32 v235, s47, v235\n" \
  "v_mov_b32 v70, 1\n" \
  "s_and_b32 s47, s36, 2\n" \
  "s_lshl_b32 s47, s47, 2\n" \
  "s_lshl_b32 s47, 0xff00ff, s47\n" \
  "s_lshl_b32 s49, 0x800, s48\n" \
  "s_and_b32 s68, s68, s47\n" \
  "s_waitcnt vmcnt(0) lgkmcnt(0)\n" \
  "s_cbranch_scc0 fbk_e9\n" \
  "s_mov_b32 exec_lo, s68\n" \
  "s_mov_b32 exec_hi, 0\n" \
  "buffer_atomic_add v70, v235, s[12:15], 0 offen\n" \
  "s_mov_b64 exec, -1\n" \
  "fbk_e9:\n" \
    \
  "s_cmp_ge_u32 s66, 8\n" \
  "s_cbranch_scc1 fbk_e6\n" \
  "s_mov_b32 exec_lo, s67\n" \
  "s_mov_b32 exec_hi, 0\n" \
  "buffer_load_dword v31, v30, s[12:15], s65 offen sc1\n" \
  "s_mov_b64 exec, -1\n" \
  "fbk_e6:\n" \
    \
  "s_mov_b32 exec_lo, 0\n" \
  "s_mov_b32 exec_hi, s49\n" \
  "s_mov_b32 s47, 0\n" \
  "v_mov_b32 v68, 0\n" \
  "v_mov_b32 v69, 0\n" \
  "s_sleep 14\n" \
  "buffer_atomic_add v69, v252, s[12:15], 0 offen sc0\n" \
    \
  "s_barrier\n" \
  "buffer_atomic_add v68, v252, s[12:15], 0 offen sc0\n" \
  "s_mov_b64 exec, -1\n" \
  "ds_write_b128 v204, v[200:203] offset:0\n" \
  "ds_write_b128 v204, v[160:163] offset:4096\n" \
  "ds_write_b128 v204, v[164:167] offset:8192\n" \
  "ds_write_b128 v204, v[168:171] offset:12288\n" \
  "ds_write_b128 v204, v[172:175] offset:16384\n" \
  "ds_write_b128 v204, v[176:179] offset:20480\n" \
  "ds_write_b128 v204, v[180:183] offset:49152\n" \
  "ds_write_b128 v204, v[184:187] offset:53248\n" \
  "ds_write_b128 v204, v[188:191] offset:57344\n" \
  "ds_write_b128 v204, v[192:195] offset:61440\n" \
  "ds_write_b128 v205, v[196:199] offset:0\n" \
  "s_mov_b32 exec_lo, 0\n" \
  "s_mov_b32 exec_hi, s49\n" \
  "fbk_p5:\n" \
  "s_waitcnt vmcnt(1)\n" \
  "v_sub_u32 v69, v69, v253\n" \
  "v_cmp_gt_u32 vcc, 14, v69\n" \
  "s_cbranch_vccz fbk_p5x\n" \
  "v_mov_b32 v69, 0\n" \
  "buffer_atomic_add v69, v252, s[12:15], 0 offen sc0\n" \
  "s_waitcnt vmcnt(1)\n" \
  "v_sub_u32 v68, v68, v253\n" \
  "v_cmp_gt_u32 vcc, 14, v68\n" \
  "s_cbranch_vccz fbk_p5x\n" \
  "v_mov_b32 v68, 0\n" \
  "buffer_atomic_add v68, v252, s[12:15], 0 offen sc0\n" \
  "s_add_u32 s47, s47, 1\n" \
  "s_cmp_lt_u32 s47, 0x100000\n" \
  "s_cbranch_scc1 fbk_p5\n" \
  "fbk_p5x:\n" \
  "s_mov_b64 exec, -1\n" \
  "s_lshr_b32 s48, s74, 14\n" \
  "s_lshl_b32 s48, s48, 10\n" \
  "s_add_u32 s48, s48, s71\n" \
  "s_lshr_b32 s49, s62, 14\n" \
  "s_lshl_b32 s49, s49, 10\n" \
  "s_add_u32 s49, s49, s71\n" \
  "s_lshr_b32 s50, s63, 11\n" \
  "s_add_u32 s50, s50, 84\n" \
  "s_lshl_b32 s50, s50, 10\n" \
  "s_add_u32 s50, s50, s71\n" \
  "buffer_load_dwordx4 v[32:35], v251, s[12:15], s48 offen offset:0 sc1\n" \
  "buffer_load_dwordx4 v[36:39], v251, s[12:15], s48 offen offset:1024 sc1\n" \
  "buffer_load_dwordx4 v[40:43], v251, s[12:15], s48 offen offset:2048 sc1\n" \
  "s_add_u32 s48, s48, 6144\n" \
  "buffer_load_dwordx4 v[44:47], v251, s[12:15], s48 offen offset:0 sc1\n" \
  "buffer_load_dwordx4 v[48:51], v251, s[12:15], s48 offen offset:1024 sc1\n" \
  "buffer_load_dwordx4 v[52:55], v251, s[12:15], s48 offen offset:2048 sc1\n" \
  "s_add_u32 s48, s48, 6144\n" \
  "buffer_load_dwordx4 v[56:59], v251, s[12:15], s48 offen offset:0 sc1\n" \
  "buffer_load_dwordx4 v[60:63], v251, s[12:15], s48 offen offset:1024 sc1\n" \
  "buffer_load_dwordx4 v[112:115], v251, s[12:15], s48 offen offset:2048 sc1\n" \
  "s_add_u32 s48, s48, 6144\n" \
  "buffer_load_dwordx4 v[116:119], v251, s[12:15], s48 offen offset:0 sc1\n" \
  "buffer_load_dwordx4 v[120:123], v251, s[12:15], s48 offen offset:1024 sc1\n" \
  "buffer_load_dwordx4 v[124:127], v251, s[12:15], s48 offen offset:2048 sc1\n" \
    \
  "s_waitcnt lgkmcnt(0)\n" \
  "s_barrier\n" \
  "s_waitcnt vmcnt(11)\n" \
  "v_mfma_f32_32x32x16_bf16 v[80:95], v[0:3], v[32:35], 0\n" \
  "s_add_u32 s48, s48, 6144\n" \
  "buffer_load_dwordx4 v[152:155], v251, s[12:15], s48 offen offset:0 sc1\n" \
  "v_mfma_f32_32x32x16_bf16 v[96:111], v[4:7], v[32:35], 0\n" \
  "s_waitcnt vmcnt(11)\n" \
  "v_mfma_f32_32x32x16_bf16 v[80:95], v[8:11], v[36:39], v[80:95]\n" \
  "ds_read_b64_tr_b16 v[128:129], v25 offset:45056\n" \
  "ds_read_b64_tr_b16 v[130:131], v25 offset:46080\n" \
  "v_mfma_f32_32x32x16_bf16 v[96:111], v[12:15], v[36:39], v[96:111]\n" \
  "ds_read_b64_tr_b16 v[132:133], v28 offset:45056\n" \
  "ds_read_b64_tr_b16 v[134:135], v28 offset:46080\n" \
  "buffer_load_dwordx4 v[156:159], v251, s[12:15], s48 offen offset:1024 sc1\n" \
  "s_waitcnt vmcnt(11)\n" \
  "v_mfma_f32_32x32x16_bf16 v[80:95], v[16:19], v[40:43], v[80:95]\n" \
  "ds_read_b64_tr_b16 v[136:137], v25 offset:49152\n" \
  "ds_read_b64_tr_b16 v[138:139], v25 offset:50176\n" \
  "v_mfma_f32_32x32x16_bf16 v[96:111], v[20:23], v[40:43], v[96:111]\n" \
  "ds_read_b64_tr_b16 v[140:141], v28 offset:49152\n" \
  "ds_read_b64_tr_b16 v[142:143], v28 offset:50176\n" \
  "s_waitcnt vmcnt(10) lgkmcnt(4)\n" \
  "v_mfma_f32_32x32x16_bf16 v[80:95], v[128:131], v[44:47], v[80:95]\n" \
  "ds_read_b64_tr_b16 v[144:145], v25 offset:53248\n" \
  "ds_read_b64_tr_b16 v[146:147], v25 offset:54272\n" \
  "buffer_load_dwordx4 v[208:211], v251, s[12:15], s48 offen offset:2048 sc1\n" \
  "v_mfma_f32_32x32x16_bf16 v[96:111], v[132:135], v[44:47], v[96:111]\n" \
  "ds_read_b64_tr_b16 v[148:149], v28 offset:53248\n" \
  "ds_read_b64_tr_b16 v[150:151], v28 offset:54272\n" \
  "s_waitcnt vmcnt(10) lgkmcnt(4)\n" \
  "v_mfma_f32_32x32x16_bf16 v[80:95], v[136:139], v[48:51], v[80:95]\n" \
  "ds_read_b64_tr_b16 v[128:129], v25 offset:57344\n" \
  "ds_read_b64_tr_b16 v[130:131], v25 offset:58368\n" \
  "v_mfma_f32_32x32x16_bf16 v[96:111], v[140:143], v[48:51], v[96:111]\n" \
  "ds_read_b64_tr_b16 v[132:133], v28 offset:57344\n" \
  "ds_read_b64_tr_b16 v[134:135], v28 offset:58368\n" \
  "s_add_u32 s48, s48, 6144\n" \
  "buffer_load_dwordx4 v[212:215], v251, s[12:15], s48 offen offset:0 sc1\n" \
  "s_waitcnt vmcnt(10) lgkmcnt(4)\n" \
  "v_mfma_f32_32x32x16_bf16 v[80:95], v[144:147], v[52:55], v[80:95]\n" \
  "ds_read_b64_tr_b16 v[136:137], v25 offset:61440\n" \
  "ds_read_b64_tr_b16 v[138:139], v25 offset:62464\n" \
  "v_mfma_f32_32x32x16_bf16 v[96:111], v[148:151], v[52:55], v[96:111]\n" \
  "ds_read_b64_tr_b16 v[140:141], v28 offset:61440\n" \
  "ds_read_b64_tr_b16 v[142:143], v28 offset:62464\n" \
  "s_waitcnt vmcnt(9) lgkmcnt(4)\n" \
  "v_mfma_f32_32x32x16_bf16 v[80:95], v[128:131], v[56:59], v[80:95]\n" \
  "ds_read_b64_tr_b16 v[144:145], v26 offset:0\n" \
  "ds_read_b64_tr_b16 v[146:147], v26 offset:1024\n" \
  "buffer_load_dwordx4 v[216:219], v251, s[12:15], s48 offen offset:1024 sc1\n" \
  "v_mfma_f32_32x32x16_bf16 v[96:111], v[132:135], v[56:59], v[96:111]\n" \
  "ds_read_b64_tr_b16 v[148:149], v29 offset:0\n" \
  "ds_read_b64_tr_b16 v[150:151], v29 offset:1024\n" \
  "s_waitcnt vmcnt(9) lgkmcnt(4)\n" \
  "v_mfma_f32_32x32x16_bf16 v[80:95], v[136:139], v[60:63], v[80:95]\n" \
  "ds_read_b64_tr_b16 v[128:129], v26 offset:4096\n" \
  "ds_read_b64_tr_b16 v[130:131], v26 offset:5120\n" \
  "v_mfma_f32_32x32x16_bf16 v[96:111], v[140:143], v[60:63], v[96:111]\n" \
  "ds_read_b64_tr_b16 v[132:133], v29 offset:4096\n" \
  "ds_read_b64_tr_b16 v[134:135], v29 offset:5120\n" \
  "buffer_load_dwordx4 v[220:223], v251, s[12:15], s48 offen offset:2048 sc1\n" \
  "s_waitcnt vmcnt(9) lgkmcnt(4)\n" \
  "v_mfma_f32_32x32x16_bf16 v[80:95], v[144:147], v[112:115], v[80:95]\n" \
  "ds_read_b64_tr_b16 v[136:137], v26 offset:8192\n" \
  "ds_read_b64_tr_b16 v[138:139], v26 offset:9216\n" \
  "v_mfma_f32_32x32x16_bf16 v[96:111], v[148:151], v[112:115], v[96:111]\n" \
  "ds_read_b64_tr_b16 v[140:141], v29 offset:8192\n" \
  "ds_read_b64_tr_b16 v[142:143], v29 offset:9216\n" \
  "s_waitcnt vmcnt(8) lgkmcnt(4)\n" \
  "v_mfma_f32_32x32x16_bf16 v[80:95], v[128:131], v[116:119], v[80:95]\n" \
  "ds_read_b64_tr_b16 v[144:145], v26 offset:12288\n" \
  "ds_read_b64_tr_b16 v[146:147], v26 offset:13312\n" \
  "s_add_u32 s48, s48, 6144\n" \
  "buffer_load_dwordx4 v[224:227], v251, s[12:15], s48 offen offset:0 sc1\n" \
  "v_mfma_f32_32x32x16_bf16 v[96:111], v[132:135], v[116:119], v[96:111]\n" \
  "ds_read_b64_tr_b16 v[148:149], v29 offset:12288\n" \
  "ds_read_b64_tr_b16 v[150:151], v29 offset:13312\n" \
  "s_waitcnt vmcnt(8) lgkmcnt(4)\n" \
  "v_mfma_f32_32x32x16_bf16 v[80:95], v[136:139], v[120:123], v[80:95]\n" \
  "ds_read_b64_tr_b16 v[128:129], v26 offset:16384\n" \
  "ds_read_b64_tr_b16 v[130:131], v26 offset:17408\n" \
  "v_mfma_f32_32x32x16_bf16 v[96:111], v[140:143], v[120:123], v[96:111]\n" \
  "ds_read_b64_tr_b16 v[132:133], v29 offset:16384\n" \
  "ds_read_b64_tr_b16 v[134:135], v29 offset:17408\n" \
  "buffer_load_dwordx4 v[228:231], v251, s[12:15], s48 offen offset:1024 sc1\n" \
  "s_waitcnt vmcnt(8) lgkmcnt(4)\n" \
  "v_mfma_f32_32x32x16_bf16 v[80:95], v[144:147], v[124:127], v[80:95]\n" \
  "ds_read_b64_tr_b16 v[136:137], v26 offset:20480\n" \
  "ds_read_b64_tr_b16 v[138:139], v26 offset:21504\n" \
  "v_mfma_f32_32x32x16_bf16 v[96:111], v[148:151], v[124:127], v[96:111]\n" \
  "ds_read_b64_tr_b16 v[140:141], v29 offset:20480\n" \
  "ds_read_b64_tr_b16 v[142:143], v29 offset:21504\n" \
  "s_waitcnt vmcnt(7) lgkmcnt(4)\n" \
  "v_mfma_f32_32x32x16_bf16 v[80:95], v[128:131], v[152:155], v[80:95]\n" \
  "ds_read_b64_tr_b16 v[144:145], v26 offset:24576\n" \
  "ds_read_b64_tr_b16 v[146:147], v26 offset:25600\n" \
  "buffer_load_dwordx4 v[160:163], v251, s[12:15], s48 offen offset:2048 sc1\n" \
  "v_mfma_f32_32x32x16_bf16 v[96:111], v[132:135], v[152:155], v[96:111]\n" \
  "ds_read_b64_tr_b16 v[148:149], v29 offset:24576\n" \
  "ds_read_b64_tr_b16 v[150:151], v29 offset:25600\n" \
  "s_waitcnt vmcnt(7) lgkmcnt(4)\n" \
  "v_mfma_f32_32x32x16_bf16 v[80:95], v[136:139], v[156:159], v[80:95]\n" \
  "ds_read_b64_tr_b16 v[128:129], v26 offset:28672\n" \
  "ds_read_b64_tr_b16 v[130:131], v26 offset:29696\n" \
  "v_mfma_f32_32x32x16_bf16 v[96:111], v[140:143], v[156:159], v[96:111]\n" \
  "ds_read_b64_tr_b16 v[132:133], v29 offset:28672\n" \
  "ds_read_b64_tr_b16 v[134:135], v29 offset:29696\n" \
  "s_waitcnt vmcnt(6) lgkmcnt(4)\n" \
  "v_mfma_f32_32x32x16_bf16 v[80:95], v[144:147], v[208:211], v[80:95]\n" \
  "ds_read_b64_tr_b16 v[136:137], v24 offset:0\n" \
  "ds_read_b64_tr_b16 v[138:139], v24 offset:1024\n" \
  "v_mfma_f32_32x32x16_bf16 v[96:111], v[148:151], v[208:211], v[96:111]\n" \
  "ds_read_b64_tr_b16 v[140:141], v27 offset:0\n" \
  "ds_read_b64_tr_b16 v[142:143], v27 offset:1024\n" \
    \
  "s_barrier\n" \
  "s_add_u32 s49, s49, 30720\n" \
  "buffer_load_dwordx4 v[164:167], v251, s[12:15], s49 offen offset:0 sc1\n" \
  "buffer_load_dwordx4 v[168:171], v251, s[12:15], s49 offen offset:1024 sc1\n" \
  "s_waitcnt vmcnt(7) lgkmcnt(4)\n" \
  "v_mfma_f32_32x32x16_bf16 v[80:95], v[128:131], v[212:215], v[80:95]\n" \
  "ds_read_b64_tr_b16 v[144:145], v24 offset:4096\n" \
  "ds_read_b64_tr_b16 v[146:147], v24 offset:5120\n" \
  "buffer_load_dwordx4 v[172:175], v251, s[12:15], s49 offen offset:2048 sc1\n" \
  "v_mfma_f32_32x32x16_bf16 v[96:111], v[132:135], v[212:215], v[96:111]\n" \
  "ds_read_b64_tr_b16 v[148:149], v27 offset:4096\n" \
  "ds_read_b64_tr_b16 v[150:151], v27 offset:5120\n" \
  "s_add_u32 s49, s49, 6144\n" \
  "buffer_load_dwordx4 v[176:179], v251, s[12:15], s49 offen offset:0 sc1\n" \
  "s_waitcnt vmcnt(8) lgkmcnt(4)\n" \
  "v_mfma_f32_32x32x16_bf16 v[80:95], v[136:139], v[216:219], v[80:95]\n" \
  "ds_read_b64_tr_b16 v[128:129], v24 offset:8192\n" \
  "ds_read_b64_tr_b16 v[130:131], v24 offset:9216\n" \
  "buffer_load_dwordx4 v[180:183], v251, s[12:15], s49 offen offset:1024 sc1\n" \
  "v_mfma_f32_32x32x16_bf16 v[96:111], v[140:143], v[216:219], v[96:111]\n" \
  "ds_read_b64_tr_b16 v[132:133], v27 offset:8192\n" \
  "ds_read_b64_tr_b16 v[134:135], v27 offset:9216\n" \
  "buffer_load_dwordx4 v[184:187], v251, s[12:15], s49 offen offset:2048 sc1\n" \
  "s_waitcnt vmcnt(9) lgkmcnt(4)\n" \
  "v_mfma_f32_32x32x16_bf16 v[80:95], v[144:147], v[220:223], v[80:95]\n" \
  "ds_read_b64_tr_b16 v[136:137], v24 offset:12288\n" \
  "ds_read_b64_tr_b16 v[138:139], v24 offset:13312\n" \
  "s_sub_u32 s49, s49, 36864\n" \
  "buffer_load_dwordx4 v[188:191], v251, s[12:15], s49 offen offset:0 sc1\n" \
  "v_mfma_f32_32x32x16_bf16 v[96:111], v[148:151], v[220:223], v[96:111]\n" \
  "ds_read_b64_tr_b16 v[140:141], v27 offset:12288\n" \
  "ds_read_b64_tr_b16 v[142:143], v27 offset:13312\n" \
  "buffer_load_dwordx4 v[192:195], v251, s[12:15], s49 offen offset:1024 sc1\n" \
  "s_waitcnt vmcnt(10) lgkmcnt(4)\n" \
  "v_mfma_f32_32x32x16_bf16 v[80:95], v[128:131], v[224:227], v[80:95]\n" \
  "ds_read_b64_tr_b16 v[144:145], v24 offset:16384\n" \
  "ds_read_b64_tr_b16 v[146:147], v24 offset:17408\n" \
  "buffer_load_dwordx4 v[196:199], v251, s[12:15], s49 offen offset:2048 sc1\n" \
  "v_mfma_f32_32x32x16_bf16 v[96:111], v[132:135], v[224:227], v[96:111]\n" \
  "ds_read_b64_tr_b16 v[148:149], v27 offset:16384\n" \
  "ds_read_b64_tr_b16 v[150:151], v27 offset:17408\n" \
  "s_add_u32 s49, s49, 6144\n" \
  "buffer_load_dwordx4 v[200:203], v251, s[12:15], s49 offen offset:0 sc1\n" \
  "s_waitcnt vmcnt(11) lgkmcnt(4)\n" \
  "v_mfma_f32_32x32x16_bf16 v[80:95], v[136:139], v[228:231], v[80:95]\n" \
  "ds_read_b64_tr_b16 v[128:129], v24 offset:20480\n" \
  "ds_read_b64_tr_b16 v[130:131], v24 offset:21504\n" \
  "v_mfma_f32_32x32x16_bf16 v[96:111], v[140:143], v[228:231], v[96:111]\n" \
  "ds_read_b64_tr_b16 v[132:133], v27 offset:20480\n" \
  "ds_read_b64_tr_b16 v[134:135], v27 offset:21504\n" \
  "buffer_load_dwordx4 v[32:35], v251, s[12:15], s49 offen offset:1024 sc1\n" \
  "s_waitcnt vmcnt(11) lgkmcnt(4)\n" \
  "v_mfma_f32_32x32x16_bf16 v[80:95], v[144:147], v[160:163], v[80:95]\n" \
  "ds_read_b64_tr_b16 v[136:137], v24 offset:49152\n" \
  "ds_read_b64_tr_b16 v[138:139], v24 offset:50176\n" \
  "v_mfma_f32_32x32x16_bf16 v[96:111], v[148:151], v[160:163], v[96:111]\n" \
  "ds_read_b64_tr_b16 v[140:141], v27 offset:49152\n" \
  "ds_read_b64_tr_b16 v[142:143], v27 offset:50176\n" \
  "s_waitcnt vmcnt(10) lgkmcnt(4)\n" \
  "v_mfma_f32_32x32x16_bf16 v[80:95], v[128:131], v[164:167], v[80:95]\n" \
  "ds_read_b64_tr_b16 v[144:145], v24 offset:53248\n" \
  "ds_read_b64_tr_b16 v[146:147], v24 offset:54272\n" \
  "v_mfma_f32_32x32x16_bf16 v[96:111], v[132:135], v[164:167], v[96:111]\n" \
  "ds_read_b64_tr_b16 v[148:149], v27 offset:53248\n" \
  "ds_read_b64_tr_b16 v[150:151], v27 offset:54272\n" \
  "buffer_load_dwordx4 v[36:39], v251, s[12:15], s49 offen offset:2048 sc1\n" \
  "s_waitcnt vmcnt(10) lgkmcnt(4)\n" \
  "v_mfma_f32_32x32x16_bf16 v[80:95], v[136:139], v[168:171], v[80:95]\n" \
  "ds_read_b64_tr_b16 v[128:129], v24 offset:57344\n" \
  "ds_read_b64_tr_b16 v[130:131], v24 offset:58368\n" \
  "v_mfma_f32_32x32x16_bf16 v[96:111], v[140:143], v[168:171], v[96:111]\n" \
  "ds_read_b64_tr_b16 v[132:133], v27 offset:57344\n" \
  "ds_read_b64_tr_b16 v[134:135], v27 offset:58368\n" \
  "s_add_u32 s49, s49, 6144\n" \
  "buffer_load_dwordx4 v[40:43], v251, s[12:15], s49 offen offset:0 sc1\n" \
  "s_waitcnt vmcnt(10) lgkmcnt(4)\n" \
  "v_mfma_f32_32x32x16_bf16 v[80:95], v[144:147], v[172:175], v[80:95]\n" \
  "ds_read_b64_tr_b16 v[136:137], v24 offset:61440\n" \
  "ds_read_b64_tr_b16 v[138:139], v24 offset:62464\n" \
  "v_mfma_f32_32x32x16_bf16 v[96:111], v[148:151], v[172:175], v[96:111]\n" \
  "ds_read_b64_tr_b16 v[140:141], v27 offset:61440\n" \
  "ds_read_b64_tr_b16 v[142:143], v27 offset:62464\n" \
  "buffer_load_dwordx4 v[44:47], v251, s[12:15], s49 offen offset:1024 sc1\n" \
  "s_waitcnt vmcnt(10) lgkmcnt(4)\n" \
  "v_mfma_f32_32x32x16_bf16 v[80:95], v[128:131], v[176:179], v[80:95]\n" \
  "ds_read_b64_tr_b16 v[144:145], v25 offset:0\n" \
  "ds_read_b64_tr_b16 v[146:147], v25 offset:1024\n" \
  "v_mfma_f32_32x32x16_bf16 v[96:111], v[132:135], v[176:179], v[96:111]\n" \
  "ds_read_b64_tr_b16 v[148:149], v28 offset:0\n" \
  "ds_read_b64_tr_b16 v[150:151], v28 offset:1024\n" \
  "buffer_load_dwordx4 v[48:51], v251, s[12:15], s49 offen offset:2048 sc1\n" \
    \
  "s_barrier\n" \
  "s_waitcnt vmcnt(10) lgkmcnt(4)\n" \
  "v_mfma_f32_32x32x16_bf16 v[80:95], v[136:139], v[180:183], v[80:95]\n" \
  "ds_read_b64_tr_b16 v[128:129], v25 offset:4096\n" \
  "ds_read_b64_tr_b16 v[130:131], v25 offset:5120\n" \
  "v_mfma_f32_32x32x16_bf16 v[96:111], v[140:143], v[180:183], v[96:111]\n" \
  "ds_read_b64_tr_b16 v[132:133], v28 offset:4096\n" \
  "ds_read_b64_tr_b16 v[134:135], v28 offset:5120\n" \
  "s_add_u32 s49, s49, 6144\n" \
  "buffer_load_dwordx4 v[52:55], v251, s[12:15], s49 offen offset:0 sc1\n" \
  "s_waitcnt vmcnt(10) lgkmcnt(4)\n" \
  "v_mfma_f32_32x32x16_bf16 v[80:95], v[144:147], v[184:187], v[80:95]\n" \
  "ds_read_b64_tr_b16 v[136:137], v24 offset:24576\n" \
  "ds_read_b64_tr_b16 v[138:139], v24 offset:25600\n" \
  "v_mfma_f32_32x32x16_bf16 v[96:111], v[148:151], v[184:187], v[96:111]\n" \
  "ds_read_b64_tr_b16 v[140:141], v27 offset:24576\n" \
  "ds_read_b64_tr_b16 v[142:143], v27 offset:25600\n" \
  "buffer_load_dwordx4 v[56:59], v251, s[12:15], s49 offen offset:1024 sc1\n" \
  "s_waitcnt vmcnt(10) lgkmcnt(4)\n" \
  "v_mfma_f32_32x32x16_bf16 v[80:95], v[128:131], v[188:191], v[80:95]\n" \
  "ds_read_b64_tr_b16 v[144:145], v24 offset:28672\n" \
  "ds_read_b64_tr_b16 v[146:147], v24 offset:29696\n" \
  "v_mfma_f32_32x32x16_bf16 v[96:111], v[132:135], v[188:191], v[96:111]\n" \
  "ds_read_b64_tr_b16 v[148:149], v27 offset:28672\n" \
  "ds_read_b64_tr_b16 v[150:151], v27 offset:29696\n" \
  "buffer_load_dwordx4 v[60:63], v251, s[12:15], s49 offen offset:2048 sc1\n" \
  "s_waitcnt vmcnt(10) lgkmcnt(4)\n" \
  "v_mfma_f32_32x32x16_bf16 v[80:95], v[136:139], v[192:195], v[80:95]\n" \
  "ds_read_b64_tr_b16 v[128:129], v24 offset:32768\n" \
  "ds_read_b64_tr_b16 v[130:131], v24 offset:33792\n" \
  "v_mfma_f32_32x32x16_bf16 v[96:111], v[140:143], v[192:195], v[96:111]\n" \
  "ds_read_b64_tr_b16 v[132:133], v27 offset:32768\n" \
  "ds_read_b64_tr_b16 v[134:135], v27 offset:33792\n" \
  "s_add_u32 s49, s49, 6144\n" \
  "buffer_load_dwordx4 v[112:115], v251, s[12:15], s49 offen offset:0 sc1\n" \
  "s_waitcnt vmcnt(10) lgkmcnt(4)\n" \
  "v_mfma_f32_32x32x16_bf16 v[80:95], v[144:147], v[196:199], v[80:95]\n" \
  "ds_read_b64_tr_b16 v[136:137], v24 offset:36864\n" \
  "ds_read_b64_tr_b16 v[138:139], v24 offset:37888\n" \
  "v_mfma_f32_32x32x16_bf16 v[96:111], v[148:151], v[196:199], v[96:111]\n" \
  "ds_read_b64_tr_b16 v[140:141], v27 offset:36864\n" \
  "ds_read_b64_tr_b16 v[142:143], v27 offset:37888\n" \
  "buffer_load_dwordx4 v[116:119], v251, s[12:15], s49 offen offset:1024 sc1\n" \
  "s_waitcnt vmcnt(10) lgkmcnt(4)\n" \
  "v_mfma_f32_32x32x16_bf16 v[80:95], v[128:131], v[200:203], v[80:95]\n" \
  "ds_read_b64_tr_b16 v[144:145], v24 offset:40960\n" \
  "ds_read_b64_tr_b16 v[146:147], v24 offset:41984\n" \
  "v_mfma_f32_32x32x16_bf16 v[96:111], v[132:135], v[200:203], v[96:111]\n" \
  "ds_read_b64_tr_b16 v[148:149], v27 offset:40960\n" \
  "ds_read_b64_tr_b16 v[150:151], v27 offset:41984\n" \
  "buffer_load_dwordx4 v[120:123], v251, s[12:15], s49 offen offset:2048 sc1\n" \
  "s_waitcnt vmcnt(10) lgkmcnt(4)\n" \
  "v_mfma_f32_32x32x16_bf16 v[80:95], v[136:139], v[32:35], v[80:95]\n" \
  "ds_read_b64_tr_b16 v[128:129], v24 offset:45056\n" \
  "ds_read_b64_tr_b16 v[130:131], v24 offset:46080\n" \
  "v_mfma_f32_32x32x16_bf16 v[96:111], v[140:143], v[32:35], v[96:111]\n" \
  "ds_read_b64_tr_b16 v[132:133], v27 offset:45056\n" \
  "ds_read_b64_tr_b16 v[134:135], v27 offset:46080\n" \
  "buffer_load_dwordx4 v[124:127], v251, s[12:15], s50 offen offset:0 sc1\n" \
  "s_waitcnt vmcnt(10) lgkmcnt(4)\n" \
  "v_mfma_f32_32x32x16_bf16 v[80:95], v[144:147], v[36:39], v[80:95]\n" \
  "ds_read_b64_tr_b16 v[136:137], v25 offset:8192\n" \
  "ds_read_b64_tr_b16 v[138:139], v25 offset:9216\n" \
  "v_mfma_f32_32x32x16_bf16 v[96:111], v[148:151], v[36:39], v[96:111]\n" \
  "ds_read_b64_tr_b16 v[140:141], v28 offset:8192\n" \
  "ds_read_b64_tr_b16 v[142:143], v28 offset:9216\n" \
  "s_waitcnt vmcnt(9) lgkmcnt(4)\n" \
  "v_mfma_f32_32x32x16_bf16 v[80:95], v[128:131], v[40:43], v[80:95]\n" \
  "ds_read_b64_tr_b16 v[144:145], v25 offset:12288\n" \
  "ds_read_b64_tr_b16 v[146:147], v25 offset:13312\n" \
  "v_mfma_f32_32x32x16_bf16 v[96:111], v[132:135], v[40:43], v[96:111]\n" \
  "ds_read_b64_tr_b16 v[148:149], v28 offset:12288\n" \
  "ds_read_b64_tr_b16 v[150:151], v28 offset:13312\n" \
  "s_waitcnt vmcnt(8) lgkmcnt(4)\n" \
  "v_mfma_f32_32x32x16_bf16 v[80:95], v[136:139], v[44:47], v[80:95]\n" \
  "ds_read_b64_tr_b16 v[128:129], v25 offset:16384\n" \
  "ds_read_b64_tr_b16 v[130:131], v25 offset:17408\n" \
  "v_mfma_f32_32x32x16_bf16 v[96:111], v[140:143], v[44:47], v[96:111]\n" \
  "ds_read_b64_tr_b16 v[132:133], v28 offset:16384\n" \
  "ds_read_b64_tr_b16 v[134:135], v28 offset:17408\n" \
  "s_waitcnt vmcnt(7) lgkmcnt(4)\n" \
  "v_mfma_f32_32x32x16_bf16 v[80:95], v[144:147], v[48:51], v[80:95]\n" \
  "ds_read_b64_tr_b16 v[136:137], v25 offset:20480\n" \
  "ds_read_b64_tr_b16 v[138:139], v25 offset:21504\n" \
  "v_mfma_f32_32x32x16_bf16 v[96:111], v[148:151], v[48:51], v[96:111]\n" \
  "ds_read_b64_tr_b16 v[140:141], v28 offset:20480\n" \
  "ds_read_b64_tr_b16 v[142:143], v28 offset:21504\n" \
  "s_waitcnt vmcnt(6) lgkmcnt(4)\n" \
  "v_mfma_f32_32x32x16_bf16 v[80:95], v[128:131], v[52:55], v[80:95]\n" \
  "ds_read_b64_tr_b16 v[144:145], v25 offset:24576\n" \
  "ds_read_b64_tr_b16 v[146:147], v25 offset:25600\n" \
  "v_mfma_f32_32x32x16_bf16 v[96:111], v[132:135], v[52:55], v[96:111]\n" \
  "ds_read_b64_tr_b16 v[148:149], v28 offset:24576\n" \
  "ds_read_b64_tr_b16 v[150:151], v28 offset:25600\n" \
  "s_waitcnt vmcnt(5) lgkmcnt(4)\n" \
  "v_mfma_f32_32x32x16_bf16 v[80:95], v[136:139], v[56:59], v[80:95]\n" \
  "ds_read_b64_tr_b16 v[128:129], v25 offset:28672\n" \
  "ds_read_b64_tr_b16 v[130:131], v25 offset:29696\n" \
  "v_mfma_f32_32x32x16_bf16 v[96:111], v[140:143], v[56:59], v[96:111]\n" \
  "ds_read_b64_tr_b16 v[132:133], v28 offset:28672\n" \
  "ds_read_b64_tr_b16 v[134:135], v28 offset:29696\n" \
  "s_waitcnt vmcnt(4) lgkmcnt(4)\n" \
  "v_mfma_f32_32x32x16_bf16 v[80:95], v[144:147], v[60:63], v[80:95]\n" \
  "ds_read_b64_tr_b16 v[136:137], v25 offset:32768\n" \
  "ds_read_b64_tr_b16 v[138:139], v25 offset:33792\n" \
  "v_mfma_f32_32x32x16_bf16 v[96:111], v[148:151], v[60:63], v[96:111]\n" \
  "ds_read_b64_tr_b16 v[140:141], v28 offset:32768\n" \
  "ds_read_b64_tr_b16 v[142:143], v28 offset:33792\n" \
  "s_waitcnt vmcnt(3) lgkmcnt(4)\n" \
  "v_mfma_f32_32x32x16_bf16 v[80:95], v[128:131], v[112:115], v[80:95]\n" \
  "ds_read_b64_tr_b16 v[144:145], v25 offset:36864\n" \
  "ds_read_b64_tr_b16 v[146:147], v25 offset:37888\n" \
  "v_mfma_f32_32x32x16_bf16 v[96:111], v[132:135], v[112:115], v[96:111]\n" \
  "ds_read_b64_tr_b16 v[148:149], v28 offset:36864\n" \
  "ds_read_b64_tr_b16 v[150:151], v28 offset:37888\n" \
  "s_waitcnt vmcnt(2) lgkmcnt(4)\n" \
  "v_mfma_f32_32x32x16_bf16 v[80:95], v[136:139], v[116:119], v[80:95]\n" \
  "ds_read_b64_tr_b16 v[128:129], v25 offset:40960\n" \
  "ds_read_b64_tr_b16 v[130:131], v25 offset:41984\n" \
  "v_mfma_f32_32x32x16_bf16 v[96:111], v[140:143], v[116:119], v[96:111]\n" \
  "ds_read_b64_tr_b16 v[132:133], v28 offset:40960\n" \
  "ds_read_b64_tr_b16 v[134:135], v28 offset:41984\n" \
  "s_waitcnt vmcnt(1) lgkmcnt(4)\n" \
  "v_mfma_f32_32x32x16_bf16 v[80:95], v[144:147], v[120:123], v[80:95]\n" \
  "v_mfma_f32_32x32x16_bf16 v[96:111], v[148:151], v[120:123], v[96:111]\n" \
  "s_waitcnt vmcnt(0) lgkmcnt(0)\n" \
  "v_mfma_f32_32x32x16_bf16 v[80:95], v[128:131], v[124:127], v[80:95]\n" \
  "v_mfma_f32_32x32x16_bf16 v[96:111], v[132:135], v[124:127], v[96:111]\n" \
  "s_nop 10\n" \
  "ds_write_b128 v245, v[80:83] offset:0\n" \
  "ds_write_b128 v245, v[84:87] offset:2112\n" \
  "ds_write_b128 v245, v[88:91] offset:4224\n" \
  "ds_write_b128 v245, v[92:95] offset:6336\n" \
  "ds_write_b128 v245, v[96:99] offset:8448\n" \
  "ds_write_b128 v245, v[100:103] offset:10560\n" \
  "ds_write_b128 v245, v[104:107] offset:12672\n" \
  "ds_write_b128 v245, v[108:111] offset:14784\n" \
  "s_branch fbk_e5\n" \
  ".p2align 12\n" \
  "fbk_e0:\n" \
    \
  "s_setprio 0\n" \
    \
  "v_add_u32 v212, s63, v251\n" \
  "v_add_u32 v213, 65536, v212\n" \
  "v_add_u32 v214, 131072, v212\n" \
  "ds_write_b128 v213, v[0:3] offset:4096\n" \
  "ds_write_b128 v212, v[4:7] offset:24576\n" \
  "ds_write_b128 v212, v[8:11] offset:28672\n" \
  "ds_write_b128 v212, v[12:15] offset:32768\n" \
  "ds_write_b128 v212, v[16:19] offset:36864\n" \
  "ds_write_b128 v212, v[20:23] offset:40960\n" \
  "ds_write_b128 v212, v[24:27] offset:45056\n" \
  "ds_write_b128 v213, v[28:31] offset:8192\n" \
  "ds_write_b128 v213, v[32:35] offset:12288\n" \
  "ds_write_b128 v213, v[36:39] offset:16384\n" \
  "ds_write_b128 v213, v[40:43] offset:20480\n" \
  "ds_write_b128 v213, v[44:47] offset:24576\n" \
  "v_lshrrev_b32 v204, 3, v250\n" \
  "v_and_b32 v205, 7, v250\n" \
  "v_lshl_add_u32 v206, s36, 3, v204\n" \
  "v_mul_u32_u24 v64, 2112, v205\n" \
  "v_lshl_add_u32 v64, v206, 4, v64\n" \
  "v_add_u32 v64, 110592, v64\n" \
  "v_lshlrev_b32 v205, 4, v205\n" \
  "v_lshl_or_b32 v65, v204, 10, v205\n" \
  "s_cmp_eq_u32 s66, 7\n" \
  "s_cbranch_scc1 fbk_e7\n" \
  "s_waitcnt vmcnt(0)\n" \
  "s_barrier\n" \
  "s_cmp_lg_u32 s36, 7\n" \
  "s_cbranch_scc1 fbk_e2\n" \
  "s_branch fbk_e1\n" \
  "fbk_e7:\n" \
  "v_mov_b32 v204, 1\n" \
  "s_setprio 3\n" \
  "s_waitcnt vmcnt(0) lgkmcnt(0)\n" \
  "s_mov_b32 exec_lo, s69\n" \
  "s_mov_b32 exec_hi, 0\n" \
  "s_barrier\n" \
  "buffer_atomic_add v204, v235, s[12:15], 0 offen\n" \
  "s_mov_b64 exec, -1\n" \
  "fbk_e1:\n" \
  "s_mov_b32 exec_lo, 0\n" \
  "s_mov_b32 exec_hi, 0x400\n" \
  "s_mov_b32 s47, 0\n" \
  "v_mov_b32 v204, 0\n" \
  "v_mov_b32 v205, 0\n" \
  "v_mov_b32 v206, 0\n" \
  "buffer_atomic_add v204, v252, s[12:15], 0 offen sc0\n" \
  "s_sleep 4\n" \
  "buffer_atomic_add v205, v252, s[12:15], 0 offen sc0\n" \
  "s_sleep 4\n" \
  "buffer_atomic_add v206, v252, s[12:15], 0 offen sc0\n" \
  "s_waitcnt vmcnt(2)\n" \
  "fbk_p4_loop:\n" \
  "v_sub_u32 v204, v204, v253\n" \
  "v_cmp_gt_u32 vcc, 15, v204\n" \
  "s_cbranch_vccz fbk_p4_ok\n" \
  "v_mov_b32 v204, 0\n" \
  "buffer_atomic_add v204, v252, s[12:15], 0 offen sc0\n" \
  "s_waitcnt vmcnt(2)\n" \
  "v_sub_u32 v205, v205, v253\n" \
  "v_cmp_gt_u32 vcc, 15, v205\n" \
  "s_cbranch_vccz fbk_p4_ok\n" \
  "v_mov_b32 v205, 0\n" \
  "buffer_atomic_add v205, v252, s[12:15], 0 offen sc0\n" \
  "s_waitcnt vmcnt(2)\n" \
  "v_sub_u32 v206, v206, v253\n" \
  "v_cmp_gt_u32 vcc, 15, v206\n" \
  "s_cbranch_vccz fbk_p4_ok\n" \
  "v_mov_b32 v206, 0\n" \
  "buffer_atomic_add v206, v252, s[12:15], 0 offen sc0\n" \
  "s_waitcnt vmcnt(2)\n" \
  "s_add_u32 s47, s47, 1\n" \
  "s_cmp_lt_u32 s47, 0x80000\n" \
  "s_cbranch_scc1 fbk_p4_loop\n" \
  "fbk_p4_ok:\n" \
  "s_waitcnt lgkmcnt(0)\n" \
  "s_barrier\n" \
  "s_mov_b64 exec, -1\n" \
  "s_setprio 0\n" \
  "fbk_e2:\n" \
  "ds_write_b128 v213, v[48:51] offset:28672\n" \
  "ds_write_b128 v213, v[52:55] offset:32768\n" \
  "ds_write_b128 v213, v[56:59] offset:36864\n" \
  "ds_write_b128 v213, v[60:63] offset:40960\n" \
  "s_cmp_eq_u32 s36, 7\n" \
  "s_cbranch_scc1 fbk_e3\n" \
  "s_barrier\n" \
  "fbk_e3:\n" \
  "s_waitcnt lgkmcnt(0)\n" \
  "s_barrier\n" \
  "s_waitcnt vmcnt(0)\n" \
  "fbk_e5:\n" \
  "s_waitcnt lgkmcnt(0)\n" \
  "s_barrier\n" \
  "ds_read_b128 v[204:207], v64\n" \
  "ds_read_b128 v[212:215], v64 offset:17408\n" \
  "ds_read_b128 v[208:211], v64 offset:1024\n" \
  "ds_read_b128 v[216:219], v64 offset:18432\n" \
  "s_waitcnt lgkmcnt(3)\n" \
  "v_pk_add_f32 v[236:237], v[236:237], v[204:205]\n" \
  "v_pk_add_f32 v[238:239], v[238:239], v[206:207]\n" \
  "s_waitcnt lgkmcnt(2)\n" \
  "v_pk_add_f32 v[236:237], v[236:237], v[212:213]\n" \
  "v_pk_add_f32 v[238:239], v[238:239], v[214:215]\n" \
  "s_waitcnt lgkmcnt(1)\n" \
  "v_pk_add_f32 v[240:241], v[240:241], v[208:209]\n" \
  "v_pk_add_f32 v[242:243], v[242:243], v[210:211]\n" \
  "s_waitcnt lgkmcnt(0)\n" \
  "v_pk_add_f32 v[240:241], v[240:241], v[216:217]\n" \
  "v_pk_add_f32 v[242:243], v[242:243], v[218:219]\n" \
  "v_cvt_pk_bf16_f32 v220, v236, v237\n" \
  "v_cvt_pk_bf16_f32 v221, v238, v239\n" \
  "v_cvt_pk_bf16_f32 v222, v240, v241\n" \
  "v_cvt_pk_bf16_f32 v223, v242, v243\n" \
  "buffer_store_dwordx4 v[220:223], v65, s[28:31], s82 offen\n" \
  "s_endpgm\n" \
    \
  "fbk_d6:\n" \
    \
  "s_lshl3_add_u32 s47, s38, s36\n" \
  "s_sub_u32 s47, s47, 232\n" \
  "s_cmp_ge_u32 s47, 12\n" \
  "s_cselect_b32 s48, 60, 30\n" \
  "s_add_u32 s47, s47, s48\n" \
  "s_lshl_b32 s47, s47, 14\n" \
  "v_lshlrev_b32 v204, 7, v250\n" \
  "s_sleep 4\n" \
  "buffer_load_dword v205, v204, s[24:27], s47 offen\n" \
  "s_add_u32 s47, s47, 8192\n" \
  "buffer_load_dword v206, v204, s[24:27], s47 offen\n" \
  "s_waitcnt vmcnt(0)\n" \
  "s_barrier\n" \
  "s_cmp_lt_u32 s36, 4\n" \
  "s_cbranch_scc1 fbk_d11\n" \
  "s_barrier\n" \
  "s_branch fbk_e0\n" \
  "fbk_d11:\n" \
  "s_add_u32 s48, s74, 507904\n" \
  "buffer_load_dwordx4 v[200:203], v254, s[24:27], s48 offen\n" \
  "s_add_u32 s48, s74, 524288\n" \
  "buffer_load_dwordx4 v[160:163], v254, s[24:27], s48 offen\n" \
  "s_add_u32 s48, s74, 589824\n" \
  "buffer_load_dwordx4 v[164:167], v254, s[24:27], s48 offen\n" \
  "s_add_u32 s48, s74, 606208\n" \
  "buffer_load_dwordx4 v[168:171], v254, s[24:27], s48 offen\n" \
  "s_add_u32 s48, s74, 622592\n" \
  "buffer_load_dwordx4 v[172:175], v254, s[24:27], s48 offen\n" \
  "s_add_u32 s48, s62, 491520\n" \
  "buffer_load_dwordx4 v[176:179], v254, s[24:27], s48 offen\n" \
  "s_add_u32 s48, s62, 507904\n" \
  "buffer_load_dwordx4 v[180:183], v254, s[24:27], s48 offen\n" \
  "s_add_u32 s48, s62, 524288\n" \
  "buffer_load_dwordx4 v[184:187], v254, s[24:27], s48 offen\n" \
  "s_add_u32 s48, s62, 589824\n" \
  "buffer_load_dwordx4 v[188:191], v254, s[24:27], s48 offen\n" \
  "s_add_u32 s48, s62, 606208\n" \
  "buffer_load_dwordx4 v[192:195], v254, s[24:27], s48 offen\n" \
  "s_add_u32 s48, s62, 622592\n" \
  "buffer_load_dwordx4 v[196:199], v254, s[24:27], s48 offen\n" \
  "s_waitcnt vmcnt(0)\n" \
  "s_branch fbk_d10\n" \
  "fbk_code_end:\n" \
    \
  "fbk_fallback:\n" \
  "s_waitcnt vmcnt(0)\n" \
  "v_mov_b32 v112, 0\n" \
  "v_mov_b32 v113, 0\n" \
  "v_mov_b32 v114, 0\n" \
  "v_mov_b32 v115, 0\n" \
  "v_mov_b32 v116, 0\n" \
  "v_mov_b32 v117, 0\n" \
  "v_mov_b32 v118, 0\n" \
  "v_mov_b32 v119, 0\n" \
  "v_mov_b32 v120, 0\n" \
  "v_mov_b32 v121, 0\n" \
  "v_mov_b32 v122, 0\n" \
  "v_mov_b32 v123, 0\n" \
  "v_mov_b32 v124, 0\n" \
  "v_mov_b32 v125, 0\n" \
  "v_mov_b32 v126, 0\n" \
  "v_mov_b32 v127, 0\n" \
  "v_mov_b32 v64, 0xff800000\n" \
  "s_lshl_b32 s56, s71, 17\n" \
  "s_mov_b32 s57, 64\n" \
  "fbk_f1:\n" \
  "buffer_load_dwordx4 v[180:183], v251, s[12:15], s56 offen sc1\n" \
  "s_mov_b64 exec, s[76:77]\n" \
  "buffer_load_dwordx4 v[184:187], v233, s[12:15], s56 offen sc1\n" \
  "buffer_load_dwordx4 v[188:191], v233, s[12:15], s56 offen offset:512 sc1\n" \
  "s_mov_b64 exec, -1\n" \
  "s_waitcnt vmcnt(0)\n" \
  "v_mfma_f32_32x32x16_bf16 v[128:143], v[180:183], v[172:175], 0\n" \
  "s_nop 11\n" \
  "v_max3_f32 v65, v128, v129, v64\n" \
  "v_max3_f32 v65, v130, v131, v65\n" \
  "v_max3_f32 v65, v132, v133, v65\n" \
  "v_max3_f32 v65, v134, v135, v65\n" \
  "v_max3_f32 v65, v136, v137, v65\n" \
  "v_max3_f32 v65, v138, v139, v65\n" \
  "v_max3_f32 v65, v140, v141, v65\n" \
  "v_max3_f32 v65, v142, v143, v65\n" \
  "v_mov_b32 v66, v65\n" \
  "s_nop 1\n" \
  "v_permlane32_swap_b32 v65, v66\n" \
  "v_max_f32 v65, v65, v66\n" \
  "v_sub_f32 v66, v64, v65\n" \
  "v_exp_f32 v66, v66\n" \
  "v_mov_b32 v64, v65\n" \
  "v_sub_f32 v128, v128, v65\n" \
  "v_sub_f32 v129, v129, v65\n" \
  "v_sub_f32 v130, v130, v65\n" \
  "v_sub_f32 v131, v131, v65\n" \
  "v_sub_f32 v132, v132, v65\n" \
  "v_sub_f32 v133, v133, v65\n" \
  "v_sub_f32 v134, v134, v65\n" \
  "v_sub_f32 v135, v135, v65\n" \
  "v_sub_f32 v136, v136, v65\n" \
  "v_sub_f32 v137, v137, v65\n" \
  "v_sub_f32 v138, v138, v65\n" \
  "v_sub_f32 v139, v139, v65\n" \
  "v_sub_f32 v140, v140, v65\n" \
  "v_sub_f32 v141, v141, v65\n" \
  "v_sub_f32 v142, v142, v65\n" \
  "v_sub_f32 v143, v143, v65\n" \
  "v_exp_f32 v128, v128\n" \
  "v_exp_f32 v129, v129\n" \
  "v_exp_f32 v130, v130\n" \
  "v_exp_f32 v131, v131\n" \
  "v_exp_f32 v132, v132\n" \
  "v_exp_f32 v133, v133\n" \
  "v_exp_f32 v134, v134\n" \
  "v_exp_f32 v135, v135\n" \
  "v_exp_f32 v136, v136\n" \
  "v_exp_f32 v137, v137\n" \
  "v_exp_f32 v138, v138\n" \
  "v_exp_f32 v139, v139\n" \
  "v_exp_f32 v140, v140\n" \
  "v_exp_f32 v141, v141\n" \
  "v_exp_f32 v142, v142\n" \
  "v_exp_f32 v143, v143\n" \
  "v_mul_f32 v112, v112, v66\n" \
  "v_mul_f32 v113, v113, v66\n" \
  "v_mul_f32 v114, v114, v66\n" \
  "v_mul_f32 v115, v115, v66\n" \
  "v_mul_f32 v116, v116, v66\n" \
  "v_mul_f32 v117, v117, v66\n" \
  "v_mul_f32 v118, v118, v66\n" \
  "v_mul_f32 v119, v119, v66\n" \
  "v_mul_f32 v120, v120, v66\n" \
  "s_nop 1\n" \
  "v_smfmac_f32_32x32x32_bf16 v[112:127], v[184:187], v[128:135], v232\n" \
  "v_smfmac_f32_32x32x32_bf16 v[112:127], v[188:191], v[136:143], v232\n" \
  "s_add_u32 s56, s56, 2048\n" \
  "s_sub_u32 s57, s57, 1\n" \
  "s_cmp_lg_u32 s57, 0\n" \
  "s_nop 8\n" \
  "s_cbranch_scc1 fbk_f1\n" \
  "v_mov_b32 v121, v120\n" \
  "s_nop 1\n" \
  "v_permlane32_swap_b32 v120, v121\n" \
  "v_add_f32 v120, v120, v121\n" \
  "v_rcp_f32 v122, v120\n" \
  "s_nop 0\n" \
  "v_pk_mul_f32 v[112:113], v[112:113], v[122:123] op_sel_hi:[1,0]\n" \
  "v_pk_mul_f32 v[114:115], v[114:115], v[122:123] op_sel_hi:[1,0]\n" \
  "v_pk_mul_f32 v[116:117], v[116:117], v[122:123] op_sel_hi:[1,0]\n" \
  "v_pk_mul_f32 v[118:119], v[118:119], v[122:123] op_sel_hi:[1,0]\n" \
  "v_cvt_pk_bf16_f32 v124, v112, v113\n" \
  "v_cvt_pk_bf16_f32 v125, v114, v115\n" \
  "v_cvt_pk_bf16_f32 v126, v116, v117\n" \
  "v_cvt_pk_bf16_f32 v127, v118, v119\n" \
  "buffer_store_dwordx4 v[124:127], v251, s[12:15], s61 offen sc1\n" \
  "s_nop 1\n" \
  "s_branch fbk_b2\n" \
  ".p2align 12\n" \
  ""
#define FBK_CLOB "v1","v2","v3","v4","v5","v6","v7","v8","v9","v10","v11","v12","v13","v14","v15","v16","v17","v18","v19","v20","v21","v22","v23","v24","v25","v26","v27","v28","v29","v30","v31","v32","v33","v34","v35","v36","v37","v38","v39","v40","v41","v42","v43","v44","v45","v46","v47","v48","v49","v50","v51","v52","v53","v54","v55","v56","v57","v58","v59","v60","v61","v62","v63","v64","v65","v66","v67","v68","v69","v70","v71","v72","v73","v74","v75","v76","v77","v78","v79","v80","v81","v82","v83","v84","v85","v86","v87","v88","v89","v90","v91","v92","v93","v94","v95","v96","v97","v98","v99","v100","v101","v102","v103","v104","v105","v106","v107","v108","v109","v110","v111","v112","v113","v114","v115","v116","v117","v118","v119","v120","v121","v122","v123","v124","v125","v126","v127","v128","v129","v130","v131","v132","v133","v134","v135","v136","v137","v138","v139","v140","v141","v142","v143","v144","v145","v146","v147","v148","v149","v150","v151","v152","v153","v154","v155","v156","v157","v158","v159","v160","v161","v162","v163","v164","v165","v166","v167","v168","v169","v170","v171","v172","v173","v174","v175","v176","v177","v178","v179","v180","v181","v182","v183","v184","v185","v186","v187","v188","v189","v190","v191","v192","v193","v194","v195","v196","v197","v198","v199","v200","v201","v202","v203","v204","v205","v206","v207","v208","v209","v210","v211","v212","v213","v214","v215","v216","v217","v218","v219","v220","v221","v222","v223","v224","v225","v226","v227","v228","v229","v230","v231","v232","v233","v234","v235","v236","v237","v238","v239","v240","v241","v242","v243","v244","v245","v246","v247","v248","v249","v250","v251","v252","v253","v254","v255","s3","s4","s5","s6","s7","s8","s9","s10","s11","s12","s13","s14","s15","s16","s17","s18","s19","s20","s21","s22","s23","s24","s25","s26","s27","s28","s29","s30","s31","s32","s33","s34","s35","s36","s37","s38","s39","s40","s41","s42","s43","s44","s45","s46","s47","s48","s49","s50","s51","s52","s53","s54","s55","s56","s57","s58","s59","s60","s61","s62","s63","s64","s65","s66","s67","s68","s69","s70","s71","s72","s73","s74","s75","s76","s77","s78","s79","s81","s82","s83","s84","s85","s86","s87","s88","s89","s90","s91","s92","s93","s94"

__global__ void __launch_bounds__(512, 2)
fused_block_kernel(const char* gx, const char* gwqkv, const char* gwo, const char* gw1, const char* gw3, const char* gw2, char* gout, char* ws) {
#if defined(__HIP_DEVICE_COMPILE__)
  __shared__ __attribute__((aligned(16))) char lds_all[163840];
  unsigned tid = __builtin_amdgcn_workitem_id_x();
  asm volatile(FBK_ASM
               : "+{v0}"(tid)
               : "{s[0:1]}"(__builtin_amdgcn_kernarg_segment_ptr()), "{s2}"(__builtin_amdgcn_workgroup_id_x()),
                 "{s80}"((unsigned)(size_t)(__attribute__((address_space(3))) char*)lds_all)
               : "memory", "scc", "vcc", FBK_CLOB);
#endif
}

extern "C" void kernel_launch(void* const* d_in, const int* in_sizes, int n_in, void* d_out, int out_size, void* d_ws, size_t ws_size,
                              hipStream_t stream) {
  (void)in_sizes; (void)n_in; (void)out_size; (void)ws_size;
  fused_block_kernel<<<256, 512, 0, stream>>>((const char*)d_in[0], (const char*)d_in[1], (const char*)d_in[2], (const char*)d_in[4],
                                              (const char*)d_in[5], (const char*)d_in[6], (char*)d_out, (char*)d_ws);
}
```
